```python
import torch

def _softmax(x, axis=-1):
    return torch.softmax(x, dim=axis)
def _gelu(x):
    return torch.nn.functional.gelu(x, approximate="tanh")
def _silu(x):
    return torch.nn.functional.silu(x)
def _sigmoid(x):
    return torch.sigmoid(x)
def _layernorm(x, axis=-1, eps=1e-5):
    m = x.mean(dim=axis, keepdim=True)
    v = x.var(dim=axis, keepdim=True, unbiased=False)
    return (x - m) / torch.sqrt(v + eps)
def _scatter_add(tgt, idx, src, axis=0):
    return tgt.clone().index_add_(axis, idx, src)
def _segment_sum(src, seg, n):
    out = torch.zeros((n,) + src.shape[1:], dtype=src.dtype)
    return out.index_add_(0, seg, src)
def _pad(x, pw):
    flat = tuple(v for pair in reversed(pw) for v in pair)
    return torch.nn.functional.pad(x, flat)

kernel_name = 'parallel_sum__sliding_block_cached__geglu_ffn__decode'

def setup_inputs():

    _w = (lambda a: (lambda t: t.to(torch.bfloat16) if t.is_floating_point() else t)(torch.as_tensor(a)))
    def _A_setup():
        B, Sq, D = 2, 1, 512
        Hq, Hkv, Sk, Dh = 32, 32, 4096, 128
        assert Hq % Hkv == 0, (Hq, Hkv)
        return {
            "x": _w((lambda *s: torch.randn(*s, dtype=torch.bfloat16) * 0.05)(B, Sq, D)),
            "k_cache": _w((lambda *s: torch.randn(*s, dtype=torch.bfloat16) * 3.2)(B, Hkv, Sk, Dh)),
            "v_cache": _w((lambda *s: torch.randn(*s, dtype=torch.bfloat16) * 1.6)(B, Hkv, Sk, Dh)),
            "wq": _w((lambda *s: torch.randn(*s, dtype=torch.bfloat16) * 3.2)(D, Hq * Dh) * (D ** -0.5)),
            "wo": _w((lambda *s: torch.randn(*s, dtype=torch.bfloat16) * 1.6)(Hq * Dh, D) * ((Hq * Dh) ** -0.5)),
        }
    _A_in = _A_setup()

    def _B_setup():
        B, S, D, F = 2, 1, 512, 1376
        return {
            "x": _w((lambda *s: torch.randn(*s, dtype=torch.bfloat16) * 0.05)(B, S, D)),
            "w_gate": _w((lambda *s: torch.randn(*s, dtype=torch.bfloat16) * 0.05)(D, F)),
            "w_up": _w((lambda *s: torch.randn(*s, dtype=torch.bfloat16) * 0.05)(D, F)),
            "w_down": _w((lambda *s: torch.randn(*s, dtype=torch.bfloat16) * 0.05)(F, D)),
        }
    _B_in = _B_setup()

    return {
        "A_x": _A_in["x"],
        "A_k_cache": _A_in["k_cache"],
        "A_v_cache": _A_in["v_cache"],
        "A_wq": _A_in["wq"],
        "A_wo": _A_in["wo"],
        "B_x": _B_in["x"],
        "B_w_gate": _B_in["w_gate"],
        "B_w_up": _B_in["w_up"],
        "B_w_down": _B_in["w_down"],
    }

def reference(A_x, A_k_cache, A_v_cache, A_wq, A_wo, B_x, B_w_gate, B_w_up, B_w_down):
    def _A_ref(x, k_cache, v_cache, wq, wo):
        B, Sq, D = x.shape
        Hq, Dh = 32, 128
        Hkv, Sk = k_cache.shape[1], k_cache.shape[2]
        W = min(4096, max(256, Sk // 4))
        rep = Hq // Hkv
        q = (lambda x, *d: x.permute(*d))((x @ wq).reshape(B, Sq, Hq, Dh), 0, 2, 1, 3)
        k = (lambda x, n, axis: torch.repeat_interleave(x, n, dim=axis))(k_cache[:, :, Sk - W:, :], rep, axis=1)
        v = (lambda x, n, axis: torch.repeat_interleave(x, n, dim=axis))(v_cache[:, :, Sk - W:, :], rep, axis=1)
        scores = torch.einsum("bhqd,bhkd->bhqk", q, k) * (Dh ** -0.5)
        p = _softmax(scores, axis=-1)
        o = torch.einsum("bhqk,bhkd->bhqd", p, v)
        return (lambda x, *d: x.permute(*d))(o, 0, 2, 1, 3).reshape(B, Sq, Hq * Dh) @ wo

    def _B_ref(x, w_gate, w_up, w_down):
        return (_gelu(x @ w_gate) * (x @ w_up)) @ w_down

    x = A_x
    xn = _layernorm(x, axis=-1)
    return x + _A_ref(xn, A_k_cache, A_v_cache, A_wq, A_wo) + _B_ref(xn, B_w_gate, B_w_up, B_w_down)
```

```cpp
#include <hip/hip_runtime.h>
#include <hip/hip_bf16.h>
#include <stdint.h>

typedef unsigned short u16;
__device__ __forceinline__ float bf2f(u16 v) { return __uint_as_float(((unsigned)v) << 16); }
__device__ __forceinline__ float rbf(float f) { __bf16 b = (__bf16)f; return (float)b; }
__device__ __forceinline__ u16 f2bf(float f) { __bf16 b = (__bf16)f; return __builtin_bit_cast(u16, b); }

constexpr int D = 512, H = 32, DH = 128, SK = 4096, W = 1024, F = 1376;

__device__ void layernorm(const u16* x, float* xn  , float* red) {
  int t = threadIdx.x;
  for (int b = 0; b < 2; ++b) {
    float a0 = bf2f(x[b * D + t]), a1 = bf2f(x[b * D + t + 256]);
    float s = a0 + a1;
    for (int o = 32; o; o >>= 1) s += __shfl_xor(s, o);
    if ((t & 63) == 0) red[t >> 6] = s;
    __syncthreads();
    float mean = (red[0] + red[1] + red[2] + red[3]) / D;
    __syncthreads();
    float d0 = a0 - mean, d1 = a1 - mean;
    float v = d0 * d0 + d1 * d1;
    for (int o = 32; o; o >>= 1) v += __shfl_xor(v, o);
    if ((t & 63) == 0) red[t >> 6] = v;
    __syncthreads();
    float var = (red[0] + red[1] + red[2] + red[3]) / D;
    __syncthreads();
    float mb = rbf(mean), vb = rbf(var);
    float den = rbf(sqrtf(rbf(vb + 1e-5f)));
    xn[b * D + t] = rbf(rbf(a0 - mb) / den);
    xn[b * D + t + 256] = rbf(rbf(a1 - mb) / den);
  }
  __syncthreads();
}

__global__ void k1(const u16* x, const u16* wq, const u16* wg, const u16* wu, float* q, float* hact) {
  __shared__ float xn[2 * D];
  __shared__ float red[4];
  layernorm(x, xn, red);
  int c = blockIdx.x * 256 + threadIdx.x;
  if (c < H * DH) {
    float a0 = 0, a1 = 0;
    for (int k = 0; k < D; ++k) { float w = bf2f(wq[(size_t)k * (H * DH) + c]); a0 += xn[k] * w; a1 += xn[D + k] * w; }
    q[c] = rbf(a0); q[H * DH + c] = rbf(a1);
  } else if (c < H * DH + F) {
    int f = c - H * DH;
    float g0 = 0, g1 = 0, u0 = 0, u1 = 0;
    for (int k = 0; k < D; ++k) {
      float w = bf2f(wg[(size_t)k * F + f]); g0 += xn[k] * w; g1 += xn[D + k] * w;
      float w2 = bf2f(wu[(size_t)k * F + f]); u0 += xn[k] * w2; u1 += xn[D + k] * w2;
    }
    g0 = rbf(g0); g1 = rbf(g1); u0 = rbf(u0); u1 = rbf(u1);
    auto gelu = [](float v) { return 0.5f * v * (1.f + tanhf(0.7978845608028654f * (v + 0.044715f * v * v * v))); };
    hact[f] = rbf(rbf(gelu(g0)) * u0); hact[F + f] = rbf(rbf(gelu(g1)) * u1);
  }
}
__global__ void k2(const u16* kc, const u16* vc, const float* q, float* o) {
  __shared__ float s[W];
  __shared__ float qs[DH];
  __shared__ float red[4];
  __shared__ float part[2][DH];
  int bh = blockIdx.x, b = bh / H, h = bh % H, t = threadIdx.x;
  if (t < DH) qs[t] = q[b * H * DH + h * DH + t];
  __syncthreads();
  const u16* kb = kc + ((size_t)bh * SK + (SK - W)) * DH;
  const u16* vb = vc + ((size_t)bh * SK + (SK - W)) * DH;
  float mx = -INFINITY;
  for (int j = t; j < W; j += 256) {
    float a = 0;
    for (int d = 0; d < DH; ++d) a += qs[d] * bf2f(kb[(size_t)j * DH + d]);
    a = rbf(rbf(a) * 0.08838834764831845f);
    s[j] = a; mx = fmaxf(mx, a);
  }
  for (int of = 32; of; of >>= 1) mx = fmaxf(mx, __shfl_xor(mx, of));
  if ((t & 63) == 0) red[t >> 6] = mx;
  __syncthreads();
  mx = fmaxf(fmaxf(red[0], red[1]), fmaxf(red[2], red[3]));
  __syncthreads();
  float sum = 0;
  for (int j = t; j < W; j += 256) { float e = expf(s[j] - mx); s[j] = e; sum += e; }
  for (int of = 32; of; of >>= 1) sum += __shfl_xor(sum, of);
  if ((t & 63) == 0) red[t >> 6] = sum;
  __syncthreads();
  sum = red[0] + red[1] + red[2] + red[3];
  for (int j = t; j < W; j += 256) s[j] = rbf(s[j] / sum);
  __syncthreads();
  int d = t & 127, half = t >> 7;
  float a = 0;
  for (int j = half * 512; j < half * 512 + 512; ++j) a += s[j] * bf2f(vb[(size_t)j * DH + d]);
  part[half][d] = a;
  __syncthreads();
  if (t < DH) o[b * H * DH + h * DH + t] = rbf(part[0][t] + part[1][t]);
}
__global__ void k3(const u16* x, const u16* wo, const u16* wd, const float* o, const float* hact, u16* out) {
  __shared__ float pa[8][2][32], pf[8][2][32];
  int c = blockIdx.x * 32 + (threadIdx.x & 31), sl = threadIdx.x >> 5;
  float a0 = 0, a1 = 0, f0 = 0, f1 = 0;
  for (int k = sl; k < H * DH; k += 8) { float w = bf2f(wo[(size_t)k * D + c]); a0 += o[k] * w; a1 += o[H * DH + k] * w; }
  for (int k = sl; k < F; k += 8) { float w = bf2f(wd[(size_t)k * D + c]); f0 += hact[k] * w; f1 += hact[F + k] * w; }
  pa[sl][0][threadIdx.x & 31] = a0; pa[sl][1][threadIdx.x & 31] = a1;
  pf[sl][0][threadIdx.x & 31] = f0; pf[sl][1][threadIdx.x & 31] = f1;
  __syncthreads();
  if (threadIdx.x < 64) {
    int b = threadIdx.x >> 5, cc = threadIdx.x & 31;
    float a = 0, f = 0;
    for (int i = 0; i < 8; ++i) { a += pa[i][b][cc]; f += pf[i][b][cc]; }
    int col = blockIdx.x * 32 + cc;
    float r = rbf(bf2f(x[b * D + col]) + rbf(a));
    out[b * D + col] = f2bf(r + rbf(f));
  }
}

extern "C" void kernel_launch(void* const* d_in, const int* in_sizes, int n_in, void* d_out, int out_size, void* d_ws, size_t ws_size, hipStream_t stream) {
  const u16* x = (const u16*)d_in[0];
  const u16* kc = (const u16*)d_in[1];
  const u16* vc = (const u16*)d_in[2];
  const u16* wq = (const u16*)d_in[3];
  const u16* wo = (const u16*)d_in[4];
  const u16* wg = (const u16*)d_in[6];
  const u16* wu = (const u16*)d_in[7];
  const u16* wd = (const u16*)d_in[8];
  float* q = (float*)d_ws; float* hact = q + 2 * H * DH; float* o = hact + 2 * F + 64;
  k1<<<(H * DH + F + 255) / 256, 256, 0, stream>>>(x, wq, wg, wu, q, hact);
  k2<<<64, 256, 0, stream>>>(kc, vc, q, o);
  k3<<<16, 256, 0, stream>>>(x, wo, wd, o, hact, (u16*)d_out);
}
```

```cpp
#include <hip/hip_runtime.h>
#include <hip/hip_bf16.h>
#include <stdint.h>

typedef unsigned short u16;
typedef unsigned v4u __attribute__((ext_vector_type(4)));
typedef unsigned v2u __attribute__((ext_vector_type(2)));
typedef float v4f __attribute__((ext_vector_type(4)));
typedef float v2f __attribute__((ext_vector_type(2)));
typedef __bf16 bf8 __attribute__((ext_vector_type(8)));
typedef __bf16 bf2 __attribute__((ext_vector_type(2)));
#define GAS __attribute__((address_space(1)))
#define LAS __attribute__((address_space(3)))
#define SB() __builtin_amdgcn_sched_barrier(0)
#define BAR() do { SB(); asm volatile("s_waitcnt lgkmcnt(0)\n\ts_barrier" ::: "memory"); SB(); } while (0)
#define THR() do { SB(); asm volatile("s_waitcnt vmcnt(3)" ::: "memory"); SB(); } while (0)
#define QKPOS 5
#define QDELAY 36
#define FDELAY 36
#define QSPREAD 1
#define PREFETCH 1

namespace {
constexpr int DM = 512, NH = 32, DH = 128, SK = 4096, WIN = 1024, FF = 1376;
constexpr int AUX_NT = 2, AUX_SC1 = 16;
constexpr unsigned TM = 0x3ffu;
constexpr int WS_E = 0;
constexpr int WS_Q = 4096;
constexpr int WS_F = WS_Q + 32 * 4096;
constexpr int O_REC = 640;
constexpr int WS_O = WS_F + 256 * 768;
constexpr int WS_Y = WS_O + 256 * O_REC;
constexpr int WS_END = WS_Y + 256 * 512;
constexpr int WS_ST = 4 << 20;
constexpr int CODE_LINES = 72;
constexpr unsigned SPIN_CAP = 1u << 16;
constexpr int L_Q = 0, L_H = 256, L_T = 512, L_M = 528, L_L = 576, L_X = 640, L_RED = L_X + 2048, L_RED2 = L_RED + 4096, L_END = L_RED2 + 8192;

__device__ __forceinline__ float F(unsigned v) { return __uint_as_float(v); }
__device__ __forceinline__ float bflo(unsigned v) { return __uint_as_float(v << 16); }
__device__ __forceinline__ float bfhi(unsigned v) { return __uint_as_float(v & 0xffff0000u); }
__device__ __forceinline__ unsigned pk(float lo, float hi) { bf2 r = {(__bf16)lo, (__bf16)hi}; return __builtin_bit_cast(unsigned, r); }
__device__ __forceinline__ float rbf(float f) { return bflo(pk(f, f)); }
template <int C> __device__ __forceinline__ float dppf(float x) { return __builtin_bit_cast(float, __builtin_amdgcn_mov_dpp(__builtin_bit_cast(int, x), C, 0xf, 0xf, true)); }
template <int C> __device__ __forceinline__ unsigned dppu(unsigned x) { return (unsigned)__builtin_amdgcn_mov_dpp((int)x, C, 0xf, 0xf, true); }
constexpr int X1 = 0xB1, X2 = 0x4E, X7 = 0x141, X8 = 0x128, R4 = 0x124, BC = 0x150;
__device__ __forceinline__ float xrow_sum(float x) {
  auto s = __builtin_amdgcn_permlane16_swap(__float_as_uint(x), __float_as_uint(x), false, false);
  x = __uint_as_float(s[0]) + __uint_as_float(s[1]);
  auto t = __builtin_amdgcn_permlane32_swap(__float_as_uint(x), __float_as_uint(x), false, false);
  return __uint_as_float(t[0]) + __uint_as_float(t[1]);
}
__device__ __forceinline__ float xrow_max(float x) {
  auto s = __builtin_amdgcn_permlane16_swap(__float_as_uint(x), __float_as_uint(x), false, false);
  x = fmaxf(__uint_as_float(s[0]), __uint_as_float(s[1]));
  auto t = __builtin_amdgcn_permlane32_swap(__float_as_uint(x), __float_as_uint(x), false, false);
  return fmaxf(__uint_as_float(t[0]), __uint_as_float(t[1]));
}
__device__ __forceinline__ float wave_sum(float x) { x += dppf<X1>(x); x += dppf<X2>(x); x += dppf<X7>(x); x += dppf<X8>(x); return xrow_sum(x); }
__device__ __forceinline__ v4f mfma(v4u a, v4u b, v4f c) { return __builtin_amdgcn_mfma_f32_16x16x32_bf16(__builtin_bit_cast(bf8, a), __builtin_bit_cast(bf8, b), c, 0, 0, 0); }
__device__ __forceinline__ float dot2(unsigned a, unsigned b, float c) { return __builtin_amdgcn_fdot2_f32_bf16(__builtin_bit_cast(bf2, a), __builtin_bit_cast(bf2, b), c, false); }
__device__ __forceinline__ __amdgpu_buffer_rsrc_t rsrc(const void* p, int bytes) { return __builtin_amdgcn_make_buffer_rsrc((void*)p, 0, bytes, 0x00020000); }
__device__ __forceinline__ v4u ld_nt(__amdgpu_buffer_rsrc_t r, int vo, int so) { return __builtin_amdgcn_raw_buffer_load_b128(r, vo, so, AUX_NT); }
__device__ __forceinline__ v4u ld_dev(__amdgpu_buffer_rsrc_t r, int vo, int so) { return __builtin_amdgcn_raw_buffer_load_b128(r, vo, so, AUX_SC1); }
__device__ __forceinline__ v2u ld_dev2(__amdgpu_buffer_rsrc_t r, int vo, int so) { return __builtin_amdgcn_raw_buffer_load_b64(r, vo, so, AUX_SC1); }
__device__ __forceinline__ unsigned tg(float a, unsigned T) { return (__float_as_uint(a) & ~TM) | T; }
#define un(a) F((a) ^ T)
__device__ __forceinline__ void st_unit(__amdgpu_buffer_rsrc_t r, int vo, int so, v4f a, unsigned T) {
  __builtin_amdgcn_raw_buffer_store_b128(v4u{tg(a.x, T), tg(a.y, T), tg(a.z, T), tg(a.w, T)}, r, vo, so, AUX_SC1);
}
__device__ __forceinline__ unsigned bad(v4u v, unsigned T) { return (v.x ^ T) | (v.y ^ T) | (v.z ^ T) | (v.w ^ T); }
__device__ __forceinline__ unsigned bad(v2u v, unsigned T) { return (v.x ^ T) | (v.y ^ T); }
__device__ __forceinline__ bool all_ok(unsigned nb) { return __builtin_amdgcn_ballot_w64((nb & TM) != 0) == 0; }
__device__ __forceinline__ v4u sel(unsigned d, v4u mk) { return v4u{d & mk.x, d & mk.y, d & mk.z, d & mk.w}; }
__device__ __forceinline__ v4u selp(unsigned d, v4u s) { return v4u{__builtin_amdgcn_perm(d, d, s.x), __builtin_amdgcn_perm(d, d, s.y), __builtin_amdgcn_perm(d, d, s.z), __builtin_amdgcn_perm(d, d, s.w)}; }
#define STAMP(k)
#define CNT(k, v)
#define DUMP()
}

__global__ void __launch_bounds__(640) fused_decode(const u16* __restrict__ x, const u16* __restrict__ kc, const u16* __restrict__ vc,
                                                    const u16* __restrict__ wq, const u16* __restrict__ wo, const u16* __restrict__ wg,
                                                    const u16* __restrict__ wu, const u16* __restrict__ wd, u16* __restrict__ out, unsigned char* ws) {
  __shared__ __attribute__((aligned(16))) unsigned char lds_[L_END];
  LAS unsigned char* L = (LAS unsigned char*)lds_;
  STAMP(0);
  asm volatile("" :: "s"(x), "s"(kc), "s"(vc), "s"(wq), "s"(wo), "s"(wg), "s"(wu), "s"(wd), "s"(out), "s"(ws));
  const int bid = blockIdx.x, lane = threadIdx.x & 63, w = __builtin_amdgcn_readfirstlane(threadIdx.x >> 6);
  const int h = (bid & 7) * 4 + (bid >> 6), j = (bid >> 3) & 7, grp = h * 8;
  const int g = lane >> 4, c = lane & 15;
  const int oct0 = (h * 43) >> 3, noct = (((h + 1) * 43) >> 3) - oct0;
  const __amdgpu_buffer_rsrc_t rws = rsrc(ws, WS_END);
  const int qh = QSPREAD ? (bid & 6) * 4 + j : h, qc = QSPREAD ? (bid >> 6) & 1 : j & 1, qr = QSPREAD ? (bid & 1) * 2 + (bid >> 7) : j >> 1;
  v4u W[16], xa, xb;
  unsigned E;
  if (w >= 8) {
    const __amdgpu_buffer_rsrc_t rx = rsrc(x, 2 * DM * 2);
    xa = __builtin_amdgcn_raw_buffer_load_b128(rx, lane * 16, 0, 0);
    xb = __builtin_amdgcn_raw_buffer_load_b128(rx, lane * 16 + 1024, 0, 0);
    E = __builtin_amdgcn_raw_buffer_load_b32(rws, WS_E, 0, AUX_SC1);
    const GAS unsigned char* wp;
    int rs;
    if (w == 8) { wp = (const GAS unsigned char*)wq + (size_t)(qr * 128 + g * 32 + (c >> 3) * 16) * 8192 + qh * 256 + qc * 128 + (c & 7) * 16; rs = 8192; }
    else { wp = (const GAS unsigned char*)(c < 8 ? wg : wu) + (size_t)(j * 64 + g * 16) * (FF * 2) + (oct0 + min(c & 7, noct - 1)) * 16; rs = FF * 2; __builtin_amdgcn_s_sleep(2); }
#pragma unroll
    for (int i = 0; i < 16; ++i) { W[i] = __builtin_nontemporal_load((const GAS v4u*)wp); wp += rs; SB(); }
  } else if (PREFETCH && w == 0) {
    unsigned long long pc;
    asm volatile("s_getpc_b64 %0" : "=s"(pc));
    E = *(const GAS unsigned*)((pc & ~127ull) + min((bid >> 3) * 3 + (lane & 3), CODE_LINES) * 128);
  }
  STAMP(1);
  BAR();
  const int mr = (c & 7) >> 1;

  if (__builtin_expect(w < 8, 1)) {
    const size_t kvo = ((size_t)((j >> 2) * NH + h) * SK + (SK - WIN) + (j & 3) * 256) * DH;
    const __amdgpu_buffer_rsrc_t rk = rsrc(kc + kvo, 65536), rv = rsrc(vc + kvo, 65536);
    const int ko = w * 1024 + lane * 16;
    v4u K[8], V[8], WO[2], WD;
#pragma unroll
    for (int i = 0; i < 8; ++i) { if (i >= 4) THR(); K[i] = ld_nt(rk, ko, i * 8192); }
    if (PREFETCH && w == 0) asm volatile("" :: "v"(E));
#pragma unroll
    for (int i = 0; i <= QKPOS; ++i) { THR(); V[i] = ld_nt(rv, ko, i * 8192); }
    const unsigned hm = (c & 1) ? 0xffff0000u : 0x0000ffffu;
    const v4u mk = {mr == 0 ? hm : 0u, mr == 1 ? hm : 0u, mr == 2 ? hm : 0u, mr == 3 ? hm : 0u};
    STAMP(2);
    BAR();
    STAMP(3);
    const v4u q = *(LAS v4u*)(L + L_Q + c * 16);
    const unsigned T = *(LAS unsigned*)(L + L_T);
    float s[8];
#pragma unroll
    for (int i = 0; i < 8; ++i) s[i] = dot2(K[i].w, q.w, dot2(K[i].z, q.z, dot2(K[i].y, q.y, dot2(K[i].x, q.x, 0.f))));
#pragma unroll
    for (int i = 0; i < 4; ++i) { const float a = s[i] + dppf<X8>(s[i]), b = s[i + 4] + dppf<X8>(s[i + 4]); s[i] = (c & 8) ? b : a; }
#pragma unroll
    for (int i = 0; i < 2; ++i) { const float a = s[i] + dppf<X7>(s[i]), b = s[i + 2] + dppf<X7>(s[i + 2]); s[i] = (c & 4) ? b : a; }
    { const float a = s[0] + dppf<X2>(s[0]), b = s[1] + dppf<X2>(s[1]); s[0] = (c & 2) ? b : a; }
    const float dotv = s[0] + dppf<X1>(s[0]);
    const float sc = rbf(rbf(dotv) * 0.08838834764831845f) * 1.4426950408889634f;
    float mx = fmaxf(sc, dppf<X2>(sc)); mx = fmaxf(mx, dppf<X7>(mx)); mx = fmaxf(mx, dppf<X8>(mx)); mx = xrow_max(mx);
    *(LAS float*)(L + L_M + w * 4) = mx;
    if (QKPOS < 7) { THR(); V[QKPOS < 7 ? QKPOS + 1 : 7] = ld_nt(rv, ko, (QKPOS + 1) * 8192); }
    BAR();
    const v4f m0 = *(LAS v4f*)(L + L_M), m1 = *(LAS v4f*)(L + L_M + 16);
    const float M = fmaxf(fmaxf(fmaxf(m0.x, m0.y), fmaxf(m0.z, m0.w)), fmaxf(fmaxf(m1.x, m1.y), fmaxf(m1.z, m1.w)));
    const float p = __builtin_amdgcn_exp2f(sc - M);
    float l = p + dppf<X2>(p); l += dppf<X7>(l); l += dppf<X8>(l); l = xrow_sum(l);
    *(LAS float*)(L + L_L + w * 4) = l;
    const unsigned pp = pk(p, p);
#pragma unroll
    for (int i = QKPOS + 2; i < 8; ++i) { THR(); V[i] = ld_nt(rv, ko, i * 8192); }
    {
      const __amdgpu_buffer_rsrc_t ro = rsrc(wo + (size_t)h * DH * DM + j * 64, (DH * DM - j * 64) * 2);
      const int oo = (w * 16 + g * 4 + (c >> 3) * 2) * 1024 + (c & 7) * 16;
      const __amdgpu_buffer_rsrc_t rd = rsrc(wd + (size_t)oct0 * 8 * DM + j * 64, noct * 8192 - j * 128);
      THR(); WD = ld_nt(rd, (w * 8 + g * 2 + (c >> 3)) * 1024 + (c & 7) * 16, 0);
      THR(); WO[0] = ld_nt(ro, oo, 0);
      THR(); WO[1] = ld_nt(ro, oo + 1024, 0);
      SB();
    }
    STAMP(4);
    v4f o = {0.f, 0.f, 0.f, 0.f};
    o = mfma(sel(dppu<BC + 0>(pp), mk), V[0], o);
    o = mfma(sel(dppu<BC + 2>(pp), mk), V[1], o);
    o = mfma(sel(dppu<BC + 4>(pp), mk), V[2], o);
    o = mfma(sel(dppu<BC + 6>(pp), mk), V[3], o);
    o = mfma(sel(dppu<BC + 8>(pp), mk), V[4], o);
    o = mfma(sel(dppu<BC + 10>(pp), mk), V[5], o);
    o = mfma(sel(dppu<BC + 12>(pp), mk), V[6], o);
    o = mfma(sel(dppu<BC + 14>(pp), mk), V[7], o);
    if (g < 2) *(LAS v4f*)(L + L_RED + w * 512 + c * 32 + g * 16) = o;
    STAMP(5);
    BAR();
    if (w == 0) {
      const v4f l0 = *(LAS v4f*)(L + L_L), l1 = *(LAS v4f*)(L + L_L + 16);
      v4f r_[8];
#pragma unroll
      for (int u = 0; u < 8; ++u) r_[u] = *(LAS v4f*)(L + L_RED + u * 512 + (lane & 31) * 16);
      SB();
      v4f a = ((r_[0] + r_[1]) + (r_[2] + r_[3])) + ((r_[4] + r_[5]) + (r_[6] + r_[7]));
      const float Ls = ((l0.x + l0.y) + (l0.z + l0.w)) + ((l1.x + l1.y) + (l1.z + l1.w));
      const float e = M + __builtin_amdgcn_logf(Ls), eh = F(__float_as_uint(e) & ~TM);
      a *= __builtin_amdgcn_rcpf(Ls);
      if (lane == 32) a = v4f{eh, e - eh, 0.f, 0.f};
      if (lane <= 32) st_unit(rws, lane * 16, WS_O + (grp + j) * O_REC, a, T);
    }
    STAMP(6);
    v4f y0, y1;
    {
      const int hi = L_H + (w * 8 + g * 2 + (c >> 3)) * 2;
      const unsigned h0 = *(LAS u16*)(L + hi), h1 = *(LAS u16*)(L + hi + 128);
      const v4f z = {0.f, 0.f, 0.f, 0.f};
      y0 = mfma(sel(h0 | (h0 << 16), mk), WD, z);
      y1 = mfma(sel(h1 | (h1 << 16), mk), WD, z);
      asm volatile("" : "+v"(y0), "+v"(y1));
    }
    {
      const int go = (w * 8 + g * 2 + (c >> 3)) * 8, eo = (lane & 7) * O_REC + 512 + (lane & 8);
      v2u P[8], PE;
      unsigned sp_ = 0;
      for (;; ++sp_) {
        asm volatile("" ::: "memory");
        PE = ld_dev2(rws, eo, WS_O + grp * O_REC);
#pragma unroll
        for (int u = 0; u < 8; ++u) P[u] = ld_dev2(rws, go, WS_O + (grp + u) * O_REC);
        unsigned nb = bad(PE, T);
#pragma unroll
        for (int u = 0; u < 8; ++u) nb |= bad(P[u], T);
        if (all_ok(nb) || sp_ > SPIN_CAP) break;
      }
      STAMP(7);
      CNT(12, sp_);
      const float e = un(PE.x) + un(PE.y);
      float em = fmaxf(e, dppf<X1>(e)); em = fmaxf(em, dppf<X2>(em));
      float wt = __builtin_amdgcn_exp2f(e - em);
      float ws_ = wt + dppf<X1>(wt); ws_ += dppf<X2>(ws_);
      wt *= __builtin_amdgcn_rcpf(ws_);
      const float w0 = dppf<BC + 0>(wt), w1 = dppf<BC + 1>(wt), w2 = dppf<BC + 2>(wt), w3 = dppf<BC + 3>(wt);
      const float w4 = dppf<BC + 4>(wt), w5 = dppf<BC + 5>(wt), w6 = dppf<BC + 6>(wt), w7 = dppf<BC + 7>(wt);
      const float a0 = fmaf(w3, un(P[3].x), fmaf(w2, un(P[2].x), fmaf(w1, un(P[1].x), w0 * un(P[0].x))));
      const float a1 = fmaf(w3, un(P[3].y), fmaf(w2, un(P[2].y), fmaf(w1, un(P[1].y), w0 * un(P[0].y))));
      const float b0 = fmaf(w7, un(P[7].x), fmaf(w6, un(P[6].x), fmaf(w5, un(P[5].x), w4 * un(P[4].x))));
      const float b1 = fmaf(w7, un(P[7].y), fmaf(w6, un(P[6].y), fmaf(w5, un(P[5].y), w4 * un(P[4].y))));
      y0 = mfma(sel(pk(a0, a0), mk), WO[0], y0);
      y1 = mfma(sel(pk(b0, b0), mk), WO[0], y1);
      y0 = mfma(sel(pk(a1, a1), mk), WO[1], y0);
      y1 = mfma(sel(pk(b1, b1), mk), WO[1], y1);
    }
    if ((g >> 1) == (c >> 3)) {
      const int ro = L_RED2 + (w * 2 + (g >> 1)) * 512 + (c & 7) * 32 + (g & 1) * 16;
      *(LAS v4f*)(L + ro) = y0;
      *(LAS v4f*)(L + ro + 256) = y1;
    }
    STAMP(8);
    BAR();
    if (w != 0) { DUMP(); return; }
    {
      v4f r_[8];
#pragma unroll
      for (int u = 0; u < 8; ++u) r_[u] = *(LAS v4f*)(L + L_RED2 + u * 1024 + lane * 16);
      SB();
      v4f a = ((r_[0] + r_[1]) + (r_[2] + r_[3])) + ((r_[4] + r_[5]) + (r_[6] + r_[7]));
      {
        auto t0 = __builtin_amdgcn_permlane32_swap(__float_as_uint(a.x), __float_as_uint(a.x), false, false); a.x = F(t0[0]) + F(t0[1]);
        auto t1 = __builtin_amdgcn_permlane32_swap(__float_as_uint(a.y), __float_as_uint(a.y), false, false); a.y = F(t1[0]) + F(t1[1]);
        auto t2 = __builtin_amdgcn_permlane32_swap(__float_as_uint(a.z), __float_as_uint(a.z), false, false); a.z = F(t2[0]) + F(t2[1]);
        auto t3 = __builtin_amdgcn_permlane32_swap(__float_as_uint(a.w), __float_as_uint(a.w), false, false); a.w = F(t3[0]) + F(t3[1]);
      }
      if (lane < 32) st_unit(rws, lane * 16 + h * 512, WS_Y + j * 16384, a, T);
      STAMP(9);
      const int oi = (h >> 4) * DM + j * 64 + (h & 15) * 4 + (lane & 1) * 2;
      const unsigned xr = *(const GAS unsigned*)(x + oi);
      const int go = (lane >> 1) * 512 + h * 16 + (lane & 1) * 8;
      v2u P;
      unsigned sp_ = 0;
      for (;; ++sp_) {
        asm volatile("" ::: "memory");
        P = ld_dev2(rws, go, WS_Y + j * 16384);
        if (all_ok(bad(P, T)) || sp_ > SPIN_CAP) break;
      }
      STAMP(10);
      CNT(13, sp_);
      float u0 = un(P.x), u1 = un(P.y);
      u0 += dppf<X2>(u0); u1 += dppf<X2>(u1);
      u0 += dppf<R4>(u0); u1 += dppf<R4>(u1);
      u0 += dppf<X8>(u0); u1 += dppf<X8>(u1);
      u0 = xrow_sum(u0); u1 = xrow_sum(u1);
      if (lane < 2) *(GAS unsigned*)(out + oi) = pk(bflo(xr) + u0, bfhi(xr) + u1);
      if (bid == 0 && lane == 0) __builtin_amdgcn_raw_buffer_store_b32(T, rws, WS_E, 0, AUX_SC1);
      STAMP(11);
      DUMP();
    }
    return;
  }

  __builtin_amdgcn_s_setprio(3);
  asm volatile("" :: "v"(xa), "v"(xb));
  STAMP(5);
#pragma unroll
  for (int b = 0; b < 2; ++b) {
    const v4u v = b ? xb : xa;
    float f[8] = {bflo(v.x), bfhi(v.x), bflo(v.y), bfhi(v.y), bflo(v.z), bfhi(v.z), bflo(v.w), bfhi(v.w)};
    const float mu = wave_sum(((f[0] + f[1]) + (f[2] + f[3])) + ((f[4] + f[5]) + (f[6] + f[7]))) * (1.f / DM);
    float q = 0.f;
#pragma unroll
    for (int e = 0; e < 8; ++e) q = fmaf(f[e] - mu, f[e] - mu, q);
    const float var = wave_sum(q) * (1.f / DM);
    const float mean = rbf(mu), den = rbf(__builtin_amdgcn_sqrtf(rbf(rbf(var) + 1e-5f))), rd = __builtin_amdgcn_rcpf(den);
#pragma unroll
    for (int e = 0; e < 8; ++e) { const float n = rbf(f[e] - mean), t = n * rd; f[e] = fmaf(fmaf(-t, den, n), rd, t); }
    *(LAS v4u*)(L + L_X + b * 1024 + lane * 16) = v4u{pk(f[0], f[1]), pk(f[2], f[3]), pk(f[4], f[5]), pk(f[6], f[7])};
  }
  asm volatile("s_waitcnt lgkmcnt(0)" ::: "memory");
  STAMP(4);
  const unsigned T = (E + 1u) & TM;
  const unsigned z4 = 0x0c0c0c0cu, slo = (c & 1) ? 0x01000c0cu : 0x0c0c0100u, shi = (c & 1) ? 0x03020c0cu : 0x0c0c0302u;
  const v4u sl = {mr == 0 ? slo : z4, mr == 1 ? slo : z4, mr == 2 ? slo : z4, mr == 3 ? slo : z4};
  const v4u sh = {mr == 0 ? shi : z4, mr == 1 ? shi : z4, mr == 2 ? shi : z4, mr == 3 ? shi : z4};
  unsigned sp_ = 0;
  if (w == 8) {
    const int xo = L_X + qr * 256 + g * 64 + (c >> 3) * 32;
    const v4u a0 = *(LAS v4u*)(L + xo), a1 = *(LAS v4u*)(L + xo + 16), b0 = *(LAS v4u*)(L + xo + 1024), b1 = *(LAS v4u*)(L + xo + 1040);
    const unsigned xs0[8] = {a0.x, a0.y, a0.z, a0.w, a1.x, a1.y, a1.z, a1.w}, xs1[8] = {b0.x, b0.y, b0.z, b0.w, b1.x, b1.y, b1.z, b1.w};
    v4f d0 = {0.f, 0.f, 0.f, 0.f}, d1 = d0;
#pragma unroll
    for (int i = 0; i < 16; ++i) {
      d0 = mfma(selp(xs0[i >> 1], (i & 1) ? sh : sl), W[i], d0);
      d1 = mfma(selp(xs1[i >> 1], (i & 1) ? sh : sl), W[i], d1);
    }
    v4f r;
#pragma unroll
    for (int e = 0; e < 4; ++e) {
      auto t = __builtin_amdgcn_permlane32_swap(__float_as_uint(d0[e]), __float_as_uint(d1[e]), false, false);
      const float keep = lane < 32 ? F(t[0]) : F(t[1]), other = lane < 32 ? F(t[1]) : F(t[0]);
      r[e] = keep + dppf<X8>(other);
    }
    STAMP(2);
    if ((g >> 1) == (c >> 3)) st_unit(rws, (g >> 1) * 512 + (c & 7) * 32 + (g & 1) * 16, WS_Q + qh * 4096 + qr * 1024 + qc * 256, r, T);
    __builtin_amdgcn_s_sleep(QDELAY);
    STAMP(6);
    const int go = (lane >> 5) * 1024 + (lane & 31) * 16, so = WS_Q + h * 4096 + (j >> 2) * 512;
    v4u P0, P1;
    for (;; ++sp_) {
      asm volatile("" ::: "memory");
      P0 = ld_dev(rws, go, so);
      P1 = ld_dev(rws, go + 2048, so);
      if (all_ok(bad(P0, T) | bad(P1, T)) || sp_ > SPIN_CAP) break;
    }
    float qv[4];
#pragma unroll
    for (int e = 0; e < 4; ++e) {
      const float a = un(P0[e]) + un(P1[e]);
      auto t = __builtin_amdgcn_permlane32_swap(__float_as_uint(a), __float_as_uint(a), false, false);
      qv[e] = F(t[0]) + F(t[1]);
    }
    *(LAS v2u*)(L + L_Q + (lane & 31) * 8) = v2u{pk(qv[0], qv[1]), pk(qv[2], qv[3])};
    *(LAS unsigned*)(L + L_T) = T;
  } else {
    const int xo = L_X + (c >> 3) * 1024 + j * 128 + g * 32;
    const v4u a0 = *(LAS v4u*)(L + xo), a1 = *(LAS v4u*)(L + xo + 16);
    const unsigned xs[8] = {a0.x, a0.y, a0.z, a0.w, a1.x, a1.y, a1.z, a1.w};
    v4f d = {0.f, 0.f, 0.f, 0.f};
#pragma unroll
    for (int i = 0; i < 16; ++i) d = mfma(selp(xs[i >> 1], (i & 1) ? sh : sl), W[i], d);
    STAMP(2);
    const int fo = (g * 12 + (c >> 3) * 6 + min(c & 7, 5)) * 16;
    if ((c & 7) < 6) st_unit(rws, fo, WS_F + (grp + j) * 768, d, T);
    __builtin_amdgcn_s_sleep(FDELAY);
    STAMP(6);
    v4u P[8];
#pragma unroll
    for (int s = 0; s < 8; ++s) P[s] = ld_dev(rws, fo, WS_F + (grp + s) * 768);
    BAR();
    BAR();
    for (;; ++sp_) {
      unsigned nb = 0;
#pragma unroll
      for (int s = 0; s < 8; ++s) nb |= bad(P[s], T);
      if (all_ok(nb) || sp_ > SPIN_CAP) break;
      asm volatile("" ::: "memory");
#pragma unroll
      for (int s = 0; s < 8; ++s) P[s] = ld_dev(rws, fo, WS_F + (grp + s) * 768);
    }
    float hv[4];
#pragma unroll
    for (int r = 0; r < 4; ++r) {
      float a = 0.f;
#pragma unroll
      for (int s = 0; s < 8; ++s) a += un(P[s][r]);
      const float gt = rbf(a), up = dppf<X8>(gt);
      const float z = 0.7978845608028654f * (gt + 0.044715f * gt * gt * gt);
      const float ge = gt * __builtin_amdgcn_rcpf(1.f + __builtin_amdgcn_exp2f(-2.885390081777927f * z));
      hv[r] = rbf(ge) * up;
    }
    if (c < 8) *(LAS v2u*)(L + L_H + (g >> 1) * 128 + c * 16 + (g & 1) * 8) = c < noct ? v2u{pk(hv[0], hv[1]), pk(hv[2], hv[3])} : v2u{0u, 0u};
  }
  STAMP(3);
  CNT(12, sp_);
  DUMP();
}

extern "C" void kernel_launch(void* const* d_in, const int* in_sizes, int n_in, void* d_out, int out_size, void* d_ws, size_t ws_size,
                              hipStream_t stream) {
  fused_decode<<<256, 640, 0, stream>>>((const u16*)d_in[0], (const u16*)d_in[1], (const u16*)d_in[2], (const u16*)d_in[3], (const u16*)d_in[4],
                                        (const u16*)d_in[6], (const u16*)d_in[7], (const u16*)d_in[8], (u16*)d_out, (unsigned char*)d_ws);
}
```

```cpp
#define TCH_K 99
#define TCH_ROLE 0
#include <hip/hip_runtime.h>
#include <hip/hip_bf16.h>
#include <stdint.h>

typedef unsigned short u16;
typedef unsigned v4u __attribute__((ext_vector_type(4)));
typedef unsigned v2u __attribute__((ext_vector_type(2)));
typedef float v4f __attribute__((ext_vector_type(4)));
typedef float v2f __attribute__((ext_vector_type(2)));
typedef __bf16 bf8 __attribute__((ext_vector_type(8)));
typedef __bf16 bf2 __attribute__((ext_vector_type(2)));
#define GAS __attribute__((address_space(1)))
#define LAS __attribute__((address_space(3)))
#define SB() __builtin_amdgcn_sched_barrier(0)
#define BAR() do { SB(); asm volatile("s_waitcnt lgkmcnt(0)\n\ts_barrier" ::: "memory"); SB(); } while (0)
#define THR() do { SB(); asm volatile("s_waitcnt vmcnt(3)" ::: "memory"); SB(); } while (0)
#define QKPOS 5
#define QDELAY 36
#define FDELAY 36
#define QSPREAD 1
#define PREFETCH 1

namespace {
constexpr int DM = 512, NH = 32, DH = 128, SK = 4096, WIN = 1024, FF = 1376;
constexpr int AUX_NT = 2, AUX_SC1 = 16;
constexpr unsigned TM = 0x3ffu;
constexpr int WS_E = 0;
constexpr int WS_Q = 4096;
constexpr int WS_F = WS_Q + 32 * 4096;
constexpr int O_REC = 640;
constexpr int WS_O = WS_F + 256 * 768;
constexpr int WS_Y = WS_O + 256 * O_REC;
constexpr int WS_END = WS_Y + 256 * 512;
constexpr int WS_ST = 4 << 20;
constexpr int CODE_LINES = 72;
constexpr unsigned SPIN_CAP = 1u << 16;
constexpr int L_Q = 0, L_H = 256, L_T = 512, L_M = 528, L_L = 576, L_X = 640, L_RED = L_X + 2048, L_RED2 = L_RED + 4096, L_END = L_RED2 + 8192;

__device__ __forceinline__ float F(unsigned v) { return __uint_as_float(v); }
__device__ __forceinline__ float bflo(unsigned v) { return __uint_as_float(v << 16); }
__device__ __forceinline__ float bfhi(unsigned v) { return __uint_as_float(v & 0xffff0000u); }
__device__ __forceinline__ unsigned pk(float lo, float hi) { bf2 r = {(__bf16)lo, (__bf16)hi}; return __builtin_bit_cast(unsigned, r); }
__device__ __forceinline__ float rbf(float f) { return bflo(pk(f, f)); }
template <int C> __device__ __forceinline__ float dppf(float x) { return __builtin_bit_cast(float, __builtin_amdgcn_mov_dpp(__builtin_bit_cast(int, x), C, 0xf, 0xf, true)); }
template <int C> __device__ __forceinline__ unsigned dppu(unsigned x) { return (unsigned)__builtin_amdgcn_mov_dpp((int)x, C, 0xf, 0xf, true); }
constexpr int X1 = 0xB1, X2 = 0x4E, X7 = 0x141, X8 = 0x128, R4 = 0x124, BC = 0x150;
__device__ __forceinline__ float xrow_sum(float x) {
  auto s = __builtin_amdgcn_permlane16_swap(__float_as_uint(x), __float_as_uint(x), false, false);
  x = __uint_as_float(s[0]) + __uint_as_float(s[1]);
  auto t = __builtin_amdgcn_permlane32_swap(__float_as_uint(x), __float_as_uint(x), false, false);
  return __uint_as_float(t[0]) + __uint_as_float(t[1]);
}
__device__ __forceinline__ float xrow_max(float x) {
  auto s = __builtin_amdgcn_permlane16_swap(__float_as_uint(x), __float_as_uint(x), false, false);
  x = fmaxf(__uint_as_float(s[0]), __uint_as_float(s[1]));
  auto t = __builtin_amdgcn_permlane32_swap(__float_as_uint(x), __float_as_uint(x), false, false);
  return fmaxf(__uint_as_float(t[0]), __uint_as_float(t[1]));
}
__device__ __forceinline__ float wave_sum(float x) { x += dppf<X1>(x); x += dppf<X2>(x); x += dppf<X7>(x); x += dppf<X8>(x); return xrow_sum(x); }
__device__ __forceinline__ v4f mfma(v4u a, v4u b, v4f c) { return __builtin_amdgcn_mfma_f32_16x16x32_bf16(__builtin_bit_cast(bf8, a), __builtin_bit_cast(bf8, b), c, 0, 0, 0); }
__device__ __forceinline__ float dot2(unsigned a, unsigned b, float c) { return __builtin_amdgcn_fdot2_f32_bf16(__builtin_bit_cast(bf2, a), __builtin_bit_cast(bf2, b), c, false); }
__device__ __forceinline__ __amdgpu_buffer_rsrc_t rsrc(const void* p, int bytes) { return __builtin_amdgcn_make_buffer_rsrc((void*)p, 0, bytes, 0x00020000); }
__device__ __forceinline__ v4u ld_nt(__amdgpu_buffer_rsrc_t r, int vo, int so) { return __builtin_amdgcn_raw_buffer_load_b128(r, vo, so, AUX_NT); }
__device__ __forceinline__ v4u ld_dev(__amdgpu_buffer_rsrc_t r, int vo, int so) { return __builtin_amdgcn_raw_buffer_load_b128(r, vo, so, AUX_SC1); }
__device__ __forceinline__ v2u ld_dev2(__amdgpu_buffer_rsrc_t r, int vo, int so) { return __builtin_amdgcn_raw_buffer_load_b64(r, vo, so, AUX_SC1); }
__device__ __forceinline__ unsigned tg(float a, unsigned T) { return (__float_as_uint(a) & ~TM) | T; }
#define un(a) F((a) ^ T)
__device__ __forceinline__ void st_unit(__amdgpu_buffer_rsrc_t r, int vo, int so, v4f a, unsigned T) {
  __builtin_amdgcn_raw_buffer_store_b128(v4u{tg(a.x, T), tg(a.y, T), tg(a.z, T), tg(a.w, T)}, r, vo, so, AUX_SC1);
}
__device__ __forceinline__ unsigned bad(v4u v, unsigned T) { return (v.x ^ T) | (v.y ^ T) | (v.z ^ T) | (v.w ^ T); }
__device__ __forceinline__ unsigned bad(v2u v, unsigned T) { return (v.x ^ T) | (v.y ^ T); }
__device__ __forceinline__ bool all_ok(unsigned nb) { return __builtin_amdgcn_ballot_w64((nb & TM) != 0) == 0; }
__device__ __forceinline__ v4u sel(unsigned d, v4u mk) { return v4u{d & mk.x, d & mk.y, d & mk.z, d & mk.w}; }
__device__ __forceinline__ v4u selp(unsigned d, v4u s) { return v4u{__builtin_amdgcn_perm(d, d, s.x), __builtin_amdgcn_perm(d, d, s.y), __builtin_amdgcn_perm(d, d, s.z), __builtin_amdgcn_perm(d, d, s.w)}; }
__device__ __forceinline__ unsigned long long rt() { unsigned long long t; SB(); asm volatile("s_memrealtime %0\n\ts_waitcnt lgkmcnt(0)" : "=s"(t) :: "memory"); SB(); return t; }
#define STAMP(k) do { if ((k) == 0) t0_ = rt(); else if ((k) == TCH_K && (w < 8 ? 0 : w - 7) == TCH_ROLE) tk_ = rt(); } while (0)
#define CNT(k, v)
#define DUMP() do { if (w < 8) { unsigned long long a_ = rt(); if (w == 0) { asm volatile("buffer_wbl2 sc1\n\ts_waitcnt vmcnt(0)" ::: "memory"); } unsigned long long b_ = rt(); const unsigned long long u_ = t0_ + 5000 + (0); while (rt() < u_) __builtin_amdgcn_s_sleep(2); } } while (0)
}

__global__ void __launch_bounds__(640) fused_decode(const u16* __restrict__ x, const u16* __restrict__ kc, const u16* __restrict__ vc,
                                                    const u16* __restrict__ wq, const u16* __restrict__ wo, const u16* __restrict__ wg,
                                                    const u16* __restrict__ wu, const u16* __restrict__ wd, u16* __restrict__ out, unsigned char* ws) {
  __shared__ __attribute__((aligned(16))) unsigned char lds_[L_END];
  LAS unsigned char* L = (LAS unsigned char*)lds_;
  unsigned long long t0_ = 0, tk_ = 0;
  const int w = __builtin_amdgcn_readfirstlane(threadIdx.x >> 6);
#define w w_
#undef w
  STAMP(0);
  asm volatile("" :: "s"(x), "s"(kc), "s"(vc), "s"(wq), "s"(wo), "s"(wg), "s"(wu), "s"(wd), "s"(out), "s"(ws));
  const int bid = blockIdx.x, lane = threadIdx.x & 63;
  const int h = (bid & 7) * 4 + (bid >> 6), j = (bid >> 3) & 7, grp = h * 8;
  const int g = lane >> 4, c = lane & 15;
  const int oct0 = (h * 43) >> 3, noct = (((h + 1) * 43) >> 3) - oct0;
  const __amdgpu_buffer_rsrc_t rws = rsrc(ws, WS_END);
  const int qh = QSPREAD ? (bid & 6) * 4 + j : h, qc = QSPREAD ? (bid >> 6) & 1 : j & 1, qr = QSPREAD ? (bid & 1) * 2 + (bid >> 7) : j >> 1;
  v4u W[16], xa, xb;
  unsigned E;
  if (w >= 8) {
    const __amdgpu_buffer_rsrc_t rx = rsrc(x, 2 * DM * 2);
    xa = __builtin_amdgcn_raw_buffer_load_b128(rx, lane * 16, 0, 0);
    xb = __builtin_amdgcn_raw_buffer_load_b128(rx, lane * 16 + 1024, 0, 0);
    E = __builtin_amdgcn_raw_buffer_load_b32(rws, WS_E, 0, AUX_SC1);
    const GAS unsigned char* wp;
    int rs;
    if (w == 8) { wp = (const GAS unsigned char*)wq + (size_t)(qr * 128 + g * 32 + (c >> 3) * 16) * 8192 + qh * 256 + qc * 128 + (c & 7) * 16; rs = 8192; }
    else { wp = (const GAS unsigned char*)(c < 8 ? wg : wu) + (size_t)(j * 64 + g * 16) * (FF * 2) + (oct0 + min(c & 7, noct - 1)) * 16; rs = FF * 2; __builtin_amdgcn_s_sleep(2); }
#pragma unroll
    for (int i = 0; i < 16; ++i) { W[i] = __builtin_nontemporal_load((const GAS v4u*)wp); wp += rs; SB(); }
  } else if (PREFETCH && w == 0) {
    unsigned long long pc;
    asm volatile("s_getpc_b64 %0" : "=s"(pc));
    E = *(const GAS unsigned*)((pc & ~127ull) + min((bid >> 3) * 3 + (lane & 3), CODE_LINES) * 128);
  }
  STAMP(1);
  BAR();
  const int mr = (c & 7) >> 1;

  if (__builtin_expect(w < 8, 1)) {
    const size_t kvo = ((size_t)((j >> 2) * NH + h) * SK + (SK - WIN) + (j & 3) * 256) * DH;
    const __amdgpu_buffer_rsrc_t rk = rsrc(kc + kvo, 65536), rv = rsrc(vc + kvo, 65536);
    const int ko = w * 1024 + lane * 16;
    v4u K[8], V[8], WO[2], WD;
#pragma unroll
    for (int i = 0; i < 8; ++i) { if (i >= 4) THR(); K[i] = ld_nt(rk, ko, i * 8192); }
    if (PREFETCH && w == 0) asm volatile("" :: "v"(E));
#pragma unroll
    for (int i = 0; i <= QKPOS; ++i) { THR(); V[i] = ld_nt(rv, ko, i * 8192); }
    const unsigned hm = (c & 1) ? 0xffff0000u : 0x0000ffffu;
    const v4u mk = {mr == 0 ? hm : 0u, mr == 1 ? hm : 0u, mr == 2 ? hm : 0u, mr == 3 ? hm : 0u};
    STAMP(2);
    BAR();
    STAMP(3);
    const v4u q = *(LAS v4u*)(L + L_Q + c * 16);
    const unsigned T = *(LAS unsigned*)(L + L_T);
    float s[8];
#pragma unroll
    for (int i = 0; i < 8; ++i) s[i] = dot2(K[i].w, q.w, dot2(K[i].z, q.z, dot2(K[i].y, q.y, dot2(K[i].x, q.x, 0.f))));
#pragma unroll
    for (int i = 0; i < 4; ++i) { const float a = s[i] + dppf<X8>(s[i]), b = s[i + 4] + dppf<X8>(s[i + 4]); s[i] = (c & 8) ? b : a; }
#pragma unroll
    for (int i = 0; i < 2; ++i) { const float a = s[i] + dppf<X7>(s[i]), b = s[i + 2] + dppf<X7>(s[i + 2]); s[i] = (c & 4) ? b : a; }
    { const float a = s[0] + dppf<X2>(s[0]), b = s[1] + dppf<X2>(s[1]); s[0] = (c & 2) ? b : a; }
    const float dotv = s[0] + dppf<X1>(s[0]);
    const float sc = rbf(rbf(dotv) * 0.08838834764831845f) * 1.4426950408889634f;
    float mx = fmaxf(sc, dppf<X2>(sc)); mx = fmaxf(mx, dppf<X7>(mx)); mx = fmaxf(mx, dppf<X8>(mx)); mx = xrow_max(mx);
    *(LAS float*)(L + L_M + w * 4) = mx;
    if (QKPOS < 7) { THR(); V[QKPOS < 7 ? QKPOS + 1 : 7] = ld_nt(rv, ko, (QKPOS + 1) * 8192); }
    BAR();
    const v4f m0 = *(LAS v4f*)(L + L_M), m1 = *(LAS v4f*)(L + L_M + 16);
    const float M = fmaxf(fmaxf(fmaxf(m0.x, m0.y), fmaxf(m0.z, m0.w)), fmaxf(fmaxf(m1.x, m1.y), fmaxf(m1.z, m1.w)));
    const float p = __builtin_amdgcn_exp2f(sc - M);
    float l = p + dppf<X2>(p); l += dppf<X7>(l); l += dppf<X8>(l); l = xrow_sum(l);
    *(LAS float*)(L + L_L + w * 4) = l;
    const unsigned pp = pk(p, p);
#pragma unroll
    for (int i = QKPOS + 2; i < 8; ++i) { THR(); V[i] = ld_nt(rv, ko, i * 8192); }
    {
      const __amdgpu_buffer_rsrc_t ro = rsrc(wo + (size_t)h * DH * DM + j * 64, (DH * DM - j * 64) * 2);
      const int oo = (w * 16 + g * 4 + (c >> 3) * 2) * 1024 + (c & 7) * 16;
      const __amdgpu_buffer_rsrc_t rd = rsrc(wd + (size_t)oct0 * 8 * DM + j * 64, noct * 8192 - j * 128);
      THR(); WD = ld_nt(rd, (w * 8 + g * 2 + (c >> 3)) * 1024 + (c & 7) * 16, 0);
      THR(); WO[0] = ld_nt(ro, oo, 0);
      THR(); WO[1] = ld_nt(ro, oo + 1024, 0);
      SB();
    }
    STAMP(4);
    v4f o = {0.f, 0.f, 0.f, 0.f};
    o = mfma(sel(dppu<BC + 0>(pp), mk), V[0], o);
    o = mfma(sel(dppu<BC + 2>(pp), mk), V[1], o);
    o = mfma(sel(dppu<BC + 4>(pp), mk), V[2], o);
    o = mfma(sel(dppu<BC + 6>(pp), mk), V[3], o);
    o = mfma(sel(dppu<BC + 8>(pp), mk), V[4], o);
    o = mfma(sel(dppu<BC + 10>(pp), mk), V[5], o);
    o = mfma(sel(dppu<BC + 12>(pp), mk), V[6], o);
    o = mfma(sel(dppu<BC + 14>(pp), mk), V[7], o);
    if (g < 2) *(LAS v4f*)(L + L_RED + w * 512 + c * 32 + g * 16) = o;
    STAMP(5);
    BAR();
    if (w == 0) {
      const v4f l0 = *(LAS v4f*)(L + L_L), l1 = *(LAS v4f*)(L + L_L + 16);
      v4f r_[8];
#pragma unroll
      for (int u = 0; u < 8; ++u) r_[u] = *(LAS v4f*)(L + L_RED + u * 512 + (lane & 31) * 16);
      SB();
      v4f a = ((r_[0] + r_[1]) + (r_[2] + r_[3])) + ((r_[4] + r_[5]) + (r_[6] + r_[7]));
      const float Ls = ((l0.x + l0.y) + (l0.z + l0.w)) + ((l1.x + l1.y) + (l1.z + l1.w));
      const float e = M + __builtin_amdgcn_logf(Ls), eh = F(__float_as_uint(e) & ~TM);
      a *= __builtin_amdgcn_rcpf(Ls);
      if (lane == 32) a = v4f{eh, e - eh, 0.f, 0.f};
      if (lane <= 32) st_unit(rws, lane * 16, WS_O + (grp + j) * O_REC, a, T);
    }
    STAMP(6);
    v4f y0, y1;
    {
      const int hi = L_H + (w * 8 + g * 2 + (c >> 3)) * 2;
      const unsigned h0 = *(LAS u16*)(L + hi), h1 = *(LAS u16*)(L + hi + 128);
      const v4f z = {0.f, 0.f, 0.f, 0.f};
      y0 = mfma(sel(h0 | (h0 << 16), mk), WD, z);
      y1 = mfma(sel(h1 | (h1 << 16), mk), WD, z);
      asm volatile("" : "+v"(y0), "+v"(y1));
    }
    {
      const int go = (w * 8 + g * 2 + (c >> 3)) * 8, eo = (lane & 7) * O_REC + 512 + (lane & 8);
      v2u P[8], PE;
      unsigned sp_ = 0;
      for (;; ++sp_) {
        asm volatile("" ::: "memory");
        PE = ld_dev2(rws, eo, WS_O + grp * O_REC);
#pragma unroll
        for (int u = 0; u < 8; ++u) P[u] = ld_dev2(rws, go, WS_O + (grp + u) * O_REC);
        unsigned nb = bad(PE, T);
#pragma unroll
        for (int u = 0; u < 8; ++u) nb |= bad(P[u], T);
        if (all_ok(nb) || sp_ > SPIN_CAP) break;
      }
      STAMP(7);
      CNT(12, sp_);
      const float e = un(PE.x) + un(PE.y);
      float em = fmaxf(e, dppf<X1>(e)); em = fmaxf(em, dppf<X2>(em));
      float wt = __builtin_amdgcn_exp2f(e - em);
      float ws_ = wt + dppf<X1>(wt); ws_ += dppf<X2>(ws_);
      wt *= __builtin_amdgcn_rcpf(ws_);
      const float w0 = dppf<BC + 0>(wt), w1 = dppf<BC + 1>(wt), w2 = dppf<BC + 2>(wt), w3 = dppf<BC + 3>(wt);
      const float w4 = dppf<BC + 4>(wt), w5 = dppf<BC + 5>(wt), w6 = dppf<BC + 6>(wt), w7 = dppf<BC + 7>(wt);
      const float a0 = fmaf(w3, un(P[3].x), fmaf(w2, un(P[2].x), fmaf(w1, un(P[1].x), w0 * un(P[0].x))));
      const float a1 = fmaf(w3, un(P[3].y), fmaf(w2, un(P[2].y), fmaf(w1, un(P[1].y), w0 * un(P[0].y))));
      const float b0 = fmaf(w7, un(P[7].x), fmaf(w6, un(P[6].x), fmaf(w5, un(P[5].x), w4 * un(P[4].x))));
      const float b1 = fmaf(w7, un(P[7].y), fmaf(w6, un(P[6].y), fmaf(w5, un(P[5].y), w4 * un(P[4].y))));
      y0 = mfma(sel(pk(a0, a0), mk), WO[0], y0);
      y1 = mfma(sel(pk(b0, b0), mk), WO[0], y1);
      y0 = mfma(sel(pk(a1, a1), mk), WO[1], y0);
      y1 = mfma(sel(pk(b1, b1), mk), WO[1], y1);
    }
    if ((g >> 1) == (c >> 3)) {
      const int ro = L_RED2 + (w * 2 + (g >> 1)) * 512 + (c & 7) * 32 + (g & 1) * 16;
      *(LAS v4f*)(L + ro) = y0;
      *(LAS v4f*)(L + ro + 256) = y1;
    }
    STAMP(8);
    BAR();
    if (w != 0) { DUMP(); return; }
    {
      v4f r_[8];
#pragma unroll
      for (int u = 0; u < 8; ++u) r_[u] = *(LAS v4f*)(L + L_RED2 + u * 1024 + lane * 16);
      SB();
      v4f a = ((r_[0] + r_[1]) + (r_[2] + r_[3])) + ((r_[4] + r_[5]) + (r_[6] + r_[7]));
      {
        auto t0 = __builtin_amdgcn_permlane32_swap(__float_as_uint(a.x), __float_as_uint(a.x), false, false); a.x = F(t0[0]) + F(t0[1]);
        auto t1 = __builtin_amdgcn_permlane32_swap(__float_as_uint(a.y), __float_as_uint(a.y), false, false); a.y = F(t1[0]) + F(t1[1]);
        auto t2 = __builtin_amdgcn_permlane32_swap(__float_as_uint(a.z), __float_as_uint(a.z), false, false); a.z = F(t2[0]) + F(t2[1]);
        auto t3 = __builtin_amdgcn_permlane32_swap(__float_as_uint(a.w), __float_as_uint(a.w), false, false); a.w = F(t3[0]) + F(t3[1]);
      }
      if (lane < 32) st_unit(rws, lane * 16 + h * 512, WS_Y + j * 16384, a, T);
      STAMP(9);
      const int oi = (h >> 4) * DM + j * 64 + (h & 15) * 4 + (lane & 1) * 2;
      const unsigned xr = *(const GAS unsigned*)(x + oi);
      const int go = (lane >> 1) * 512 + h * 16 + (lane & 1) * 8;
      v2u P;
      unsigned sp_ = 0;
      for (;; ++sp_) {
        asm volatile("" ::: "memory");
        P = ld_dev2(rws, go, WS_Y + j * 16384);
        if (all_ok(bad(P, T)) || sp_ > SPIN_CAP) break;
      }
      STAMP(10);
      CNT(13, sp_);
      float u0 = un(P.x), u1 = un(P.y);
      u0 += dppf<X2>(u0); u1 += dppf<X2>(u1);
      u0 += dppf<R4>(u0); u1 += dppf<R4>(u1);
      u0 += dppf<X8>(u0); u1 += dppf<X8>(u1);
      u0 = xrow_sum(u0); u1 = xrow_sum(u1);
      if (lane < 2) *(GAS unsigned*)(out + oi) = pk(bflo(xr) + u0, bfhi(xr) + u1);
      if (bid == 0 && lane == 0) __builtin_amdgcn_raw_buffer_store_b32(T, rws, WS_E, 0, AUX_SC1);
      STAMP(11);
      DUMP();
    }
    return;
  }

  __builtin_amdgcn_s_setprio(3);
  asm volatile("" :: "v"(xa), "v"(xb));
  STAMP(5);
#pragma unroll
  for (int b = 0; b < 2; ++b) {
    const v4u v = b ? xb : xa;
    float f[8] = {bflo(v.x), bfhi(v.x), bflo(v.y), bfhi(v.y), bflo(v.z), bfhi(v.z), bflo(v.w), bfhi(v.w)};
    const float mu = wave_sum(((f[0] + f[1]) + (f[2] + f[3])) + ((f[4] + f[5]) + (f[6] + f[7]))) * (1.f / DM);
    float q = 0.f;
#pragma unroll
    for (int e = 0; e < 8; ++e) q = fmaf(f[e] - mu, f[e] - mu, q);
    const float var = wave_sum(q) * (1.f / DM);
    const float mean = rbf(mu), den = rbf(__builtin_amdgcn_sqrtf(rbf(rbf(var) + 1e-5f))), rd = __builtin_amdgcn_rcpf(den);
#pragma unroll
    for (int e = 0; e < 8; ++e) { const float n = rbf(f[e] - mean), t = n * rd; f[e] = fmaf(fmaf(-t, den, n), rd, t); }
    *(LAS v4u*)(L + L_X + b * 1024 + lane * 16) = v4u{pk(f[0], f[1]), pk(f[2], f[3]), pk(f[4], f[5]), pk(f[6], f[7])};
  }
  asm volatile("s_waitcnt lgkmcnt(0)" ::: "memory");
  STAMP(4);
  const unsigned T = (E + 1u) & TM;
  const unsigned z4 = 0x0c0c0c0cu, slo = (c & 1) ? 0x01000c0cu : 0x0c0c0100u, shi = (c & 1) ? 0x03020c0cu : 0x0c0c0302u;
  const v4u sl = {mr == 0 ? slo : z4, mr == 1 ? slo : z4, mr == 2 ? slo : z4, mr == 3 ? slo : z4};
  const v4u sh = {mr == 0 ? shi : z4, mr == 1 ? shi : z4, mr == 2 ? shi : z4, mr == 3 ? shi : z4};
  unsigned sp_ = 0;
  if (w == 8) {
    const int xo = L_X + qr * 256 + g * 64 + (c >> 3) * 32;
    const v4u a0 = *(LAS v4u*)(L + xo), a1 = *(LAS v4u*)(L + xo + 16), b0 = *(LAS v4u*)(L + xo + 1024), b1 = *(LAS v4u*)(L + xo + 1040);
    const unsigned xs0[8] = {a0.x, a0.y, a0.z, a0.w, a1.x, a1.y, a1.z, a1.w}, xs1[8] = {b0.x, b0.y, b0.z, b0.w, b1.x, b1.y, b1.z, b1.w};
    v4f d0 = {0.f, 0.f, 0.f, 0.f}, d1 = d0;
#pragma unroll
    for (int i = 0; i < 16; ++i) {
      d0 = mfma(selp(xs0[i >> 1], (i & 1) ? sh : sl), W[i], d0);
      d1 = mfma(selp(xs1[i >> 1], (i & 1) ? sh : sl), W[i], d1);
    }
    v4f r;
#pragma unroll
    for (int e = 0; e < 4; ++e) {
      auto t = __builtin_amdgcn_permlane32_swap(__float_as_uint(d0[e]), __float_as_uint(d1[e]), false, false);
      const float keep = lane < 32 ? F(t[0]) : F(t[1]), other = lane < 32 ? F(t[1]) : F(t[0]);
      r[e] = keep + dppf<X8>(other);
    }
    STAMP(2);
    if ((g >> 1) == (c >> 3)) st_unit(rws, (g >> 1) * 512 + (c & 7) * 32 + (g & 1) * 16, WS_Q + qh * 4096 + qr * 1024 + qc * 256, r, T);
    __builtin_amdgcn_s_sleep(QDELAY);
    STAMP(6);
    const int go = (lane >> 5) * 1024 + (lane & 31) * 16, so = WS_Q + h * 4096 + (j >> 2) * 512;
    v4u P0, P1;
    for (;; ++sp_) {
      asm volatile("" ::: "memory");
      P0 = ld_dev(rws, go, so);
      P1 = ld_dev(rws, go + 2048, so);
      if (all_ok(bad(P0, T) | bad(P1, T)) || sp_ > SPIN_CAP) break;
    }
    float qv[4];
#pragma unroll
    for (int e = 0; e < 4; ++e) {
      const float a = un(P0[e]) + un(P1[e]);
      auto t = __builtin_amdgcn_permlane32_swap(__float_as_uint(a), __float_as_uint(a), false, false);
      qv[e] = F(t[0]) + F(t[1]);
    }
    *(LAS v2u*)(L + L_Q + (lane & 31) * 8) = v2u{pk(qv[0], qv[1]), pk(qv[2], qv[3])};
    *(LAS unsigned*)(L + L_T) = T;
  } else {
    const int xo = L_X + (c >> 3) * 1024 + j * 128 + g * 32;
    const v4u a0 = *(LAS v4u*)(L + xo), a1 = *(LAS v4u*)(L + xo + 16);
    const unsigned xs[8] = {a0.x, a0.y, a0.z, a0.w, a1.x, a1.y, a1.z, a1.w};
    v4f d = {0.f, 0.f, 0.f, 0.f};
#pragma unroll
    for (int i = 0; i < 16; ++i) d = mfma(selp(xs[i >> 1], (i & 1) ? sh : sl), W[i], d);
    STAMP(2);
    const int fo = (g * 12 + (c >> 3) * 6 + min(c & 7, 5)) * 16;
    if ((c & 7) < 6) st_unit(rws, fo, WS_F + (grp + j) * 768, d, T);
    __builtin_amdgcn_s_sleep(FDELAY);
    STAMP(6);
    v4u P[8];
#pragma unroll
    for (int s = 0; s < 8; ++s) P[s] = ld_dev(rws, fo, WS_F + (grp + s) * 768);
    BAR();
    BAR();
    for (;; ++sp_) {
      unsigned nb = 0;
#pragma unroll
      for (int s = 0; s < 8; ++s) nb |= bad(P[s], T);
      if (all_ok(nb) || sp_ > SPIN_CAP) break;
      asm volatile("" ::: "memory");
#pragma unroll
      for (int s = 0; s < 8; ++s) P[s] = ld_dev(rws, fo, WS_F + (grp + s) * 768);
    }
    float hv[4];
#pragma unroll
    for (int r = 0; r < 4; ++r) {
      float a = 0.f;
#pragma unroll
      for (int s = 0; s < 8; ++s) a += un(P[s][r]);
      const float gt = rbf(a), up = dppf<X8>(gt);
      const float z = 0.7978845608028654f * (gt + 0.044715f * gt * gt * gt);
      const float ge = gt * __builtin_amdgcn_rcpf(1.f + __builtin_amdgcn_exp2f(-2.885390081777927f * z));
      hv[r] = rbf(ge) * up;
    }
    if (c < 8) *(LAS v2u*)(L + L_H + (g >> 1) * 128 + c * 16 + (g & 1) * 8) = c < noct ? v2u{pk(hv[0], hv[1]), pk(hv[2], hv[3])} : v2u{0u, 0u};
  }
  STAMP(3);
  CNT(12, sp_);
  DUMP();
}

__global__ void tiny(unsigned* p) { if (p == nullptr) __builtin_trap(); }
extern "C" void kernel_launch(void* const* d_in, const int* in_sizes, int n_in, void* d_out, int out_size, void* d_ws, size_t ws_size,
                              hipStream_t stream) {
  fused_decode<<<256, 640, 0, stream>>>((const u16*)d_in[0], (const u16*)d_in[1], (const u16*)d_in[2], (const u16*)d_in[3], (const u16*)d_in[4],
                                        (const u16*)d_in[6], (const u16*)d_in[7], (const u16*)d_in[8], (u16*)d_out, (unsigned char*)d_ws);
}
```

```cpp
#define TCH_K 99
#define TCH_ROLE 0
#include <hip/hip_runtime.h>
#include <hip/hip_bf16.h>
#include <stdint.h>

typedef unsigned short u16;
typedef unsigned v4u __attribute__((ext_vector_type(4)));
typedef unsigned v2u __attribute__((ext_vector_type(2)));
typedef float v4f __attribute__((ext_vector_type(4)));
typedef float v2f __attribute__((ext_vector_type(2)));
typedef __bf16 bf8 __attribute__((ext_vector_type(8)));
typedef __bf16 bf2 __attribute__((ext_vector_type(2)));
#define GAS __attribute__((address_space(1)))
#define LAS __attribute__((address_space(3)))
#define SB() __builtin_amdgcn_sched_barrier(0)
#define BAR() do { SB(); asm volatile("s_waitcnt lgkmcnt(0)\n\ts_barrier" ::: "memory"); SB(); } while (0)
#define THR() do { SB(); asm volatile("s_waitcnt vmcnt(3)" ::: "memory"); SB(); } while (0)
#define QKPOS 5
#define QDELAY 36
#define FDELAY 36
#define QSPREAD 1
#define PREFETCH 1

namespace {
constexpr int DM = 512, NH = 32, DH = 128, SK = 4096, WIN = 1024, FF = 1376;
constexpr int AUX_NT = 2, AUX_SC1 = 16;
constexpr unsigned TM = 0x3ffu;
constexpr int WS_E = 0;
constexpr int WS_Q = 4096;
constexpr int WS_F = WS_Q + 32 * 4096;
constexpr int O_REC = 640;
constexpr int WS_O = WS_F + 256 * 768;
constexpr int WS_Y = WS_O + 256 * O_REC;
constexpr int WS_END = WS_Y + 256 * 512;
constexpr int WS_ST = 4 << 20;
constexpr int CODE_LINES = 72;
constexpr unsigned SPIN_CAP = 1u << 16;
constexpr int L_Q = 0, L_H = 256, L_T = 512, L_M = 528, L_L = 576, L_X = 640, L_RED = L_X + 2048, L_RED2 = L_RED + 4096, L_END = L_RED2 + 8192;

__device__ __forceinline__ float F(unsigned v) { return __uint_as_float(v); }
__device__ __forceinline__ float bflo(unsigned v) { return __uint_as_float(v << 16); }
__device__ __forceinline__ float bfhi(unsigned v) { return __uint_as_float(v & 0xffff0000u); }
__device__ __forceinline__ unsigned pk(float lo, float hi) { bf2 r = {(__bf16)lo, (__bf16)hi}; return __builtin_bit_cast(unsigned, r); }
__device__ __forceinline__ float rbf(float f) { return bflo(pk(f, f)); }
template <int C> __device__ __forceinline__ float dppf(float x) { return __builtin_bit_cast(float, __builtin_amdgcn_mov_dpp(__builtin_bit_cast(int, x), C, 0xf, 0xf, true)); }
template <int C> __device__ __forceinline__ unsigned dppu(unsigned x) { return (unsigned)__builtin_amdgcn_mov_dpp((int)x, C, 0xf, 0xf, true); }
constexpr int X1 = 0xB1, X2 = 0x4E, X7 = 0x141, X8 = 0x128, R4 = 0x124, BC = 0x150;
__device__ __forceinline__ float xrow_sum(float x) {
  auto s = __builtin_amdgcn_permlane16_swap(__float_as_uint(x), __float_as_uint(x), false, false);
  x = __uint_as_float(s[0]) + __uint_as_float(s[1]);
  auto t = __builtin_amdgcn_permlane32_swap(__float_as_uint(x), __float_as_uint(x), false, false);
  return __uint_as_float(t[0]) + __uint_as_float(t[1]);
}
__device__ __forceinline__ float xrow_max(float x) {
  auto s = __builtin_amdgcn_permlane16_swap(__float_as_uint(x), __float_as_uint(x), false, false);
  x = fmaxf(__uint_as_float(s[0]), __uint_as_float(s[1]));
  auto t = __builtin_amdgcn_permlane32_swap(__float_as_uint(x), __float_as_uint(x), false, false);
  return fmaxf(__uint_as_float(t[0]), __uint_as_float(t[1]));
}
__device__ __forceinline__ float wave_sum(float x) { x += dppf<X1>(x); x += dppf<X2>(x); x += dppf<X7>(x); x += dppf<X8>(x); return xrow_sum(x); }
__device__ __forceinline__ v4f mfma(v4u a, v4u b, v4f c) { return __builtin_amdgcn_mfma_f32_16x16x32_bf16(__builtin_bit_cast(bf8, a), __builtin_bit_cast(bf8, b), c, 0, 0, 0); }
__device__ __forceinline__ float dot2(unsigned a, unsigned b, float c) { return __builtin_amdgcn_fdot2_f32_bf16(__builtin_bit_cast(bf2, a), __builtin_bit_cast(bf2, b), c, false); }
__device__ __forceinline__ __amdgpu_buffer_rsrc_t rsrc(const void* p, int bytes) { return __builtin_amdgcn_make_buffer_rsrc((void*)p, 0, bytes, 0x00020000); }
__device__ __forceinline__ v4u ld_nt(__amdgpu_buffer_rsrc_t r, int vo, int so) { return __builtin_amdgcn_raw_buffer_load_b128(r, vo, so, AUX_NT); }
__device__ __forceinline__ v4u ld_dev(__amdgpu_buffer_rsrc_t r, int vo, int so) { return __builtin_amdgcn_raw_buffer_load_b128(r, vo, so, AUX_SC1); }
__device__ __forceinline__ v2u ld_dev2(__amdgpu_buffer_rsrc_t r, int vo, int so) { return __builtin_amdgcn_raw_buffer_load_b64(r, vo, so, AUX_SC1); }
__device__ __forceinline__ unsigned tg(float a, unsigned T) { return (__float_as_uint(a) & ~TM) | T; }
#define un(a) F((a) ^ T)
__device__ __forceinline__ void st_unit(__amdgpu_buffer_rsrc_t r, int vo, int so, v4f a, unsigned T) {
  __builtin_amdgcn_raw_buffer_store_b128(v4u{tg(a.x, T), tg(a.y, T), tg(a.z, T), tg(a.w, T)}, r, vo, so, AUX_SC1);
}
__device__ __forceinline__ unsigned bad(v4u v, unsigned T) { return (v.x ^ T) | (v.y ^ T) | (v.z ^ T) | (v.w ^ T); }
__device__ __forceinline__ unsigned bad(v2u v, unsigned T) { return (v.x ^ T) | (v.y ^ T); }
__device__ __forceinline__ bool all_ok(unsigned nb) { return __builtin_amdgcn_ballot_w64((nb & TM) != 0) == 0; }
__device__ __forceinline__ v4u sel(unsigned d, v4u mk) { return v4u{d & mk.x, d & mk.y, d & mk.z, d & mk.w}; }
__device__ __forceinline__ v4u selp(unsigned d, v4u s) { return v4u{__builtin_amdgcn_perm(d, d, s.x), __builtin_amdgcn_perm(d, d, s.y), __builtin_amdgcn_perm(d, d, s.z), __builtin_amdgcn_perm(d, d, s.w)}; }
__device__ __forceinline__ unsigned long long rt() { unsigned long long t; SB(); asm volatile("s_memrealtime %0\n\ts_waitcnt lgkmcnt(0)" : "=s"(t) :: "memory"); SB(); return t; }
#define STAMP(k) do { if ((k) == 0) t0_ = rt(); else if ((k) == TCH_K && (w < 8 ? 0 : w - 7) == TCH_ROLE) tk_ = rt(); } while (0)
#define CNT(k, v)
#define DUMP() do { if (w < 8) { const unsigned long long u_ = t0_ + 5000; while (rt() < u_) __builtin_amdgcn_s_sleep(2); } } while (0)
}

__global__ void __launch_bounds__(640) fused_decode(const u16* __restrict__ x, const u16* __restrict__ kc, const u16* __restrict__ vc,
                                                    const u16* __restrict__ wq, const u16* __restrict__ wo, const u16* __restrict__ wg,
                                                    const u16* __restrict__ wu, const u16* __restrict__ wd, u16* __restrict__ out, unsigned char* ws) {
  __shared__ __attribute__((aligned(16))) unsigned char lds_[L_END];
  LAS unsigned char* L = (LAS unsigned char*)lds_;
  unsigned long long t0_ = 0, tk_ = 0;
  const int w = __builtin_amdgcn_readfirstlane(threadIdx.x >> 6);
#define w w_
#undef w
  STAMP(0);
  asm volatile("" :: "s"(x), "s"(kc), "s"(vc), "s"(wq), "s"(wo), "s"(wg), "s"(wu), "s"(wd), "s"(out), "s"(ws));
  const int bid = blockIdx.x, lane = threadIdx.x & 63;
  const int h = (bid & 7) * 4 + (bid >> 6), j = (bid >> 3) & 7, grp = h * 8;
  const int g = lane >> 4, c = lane & 15;
  const int oct0 = (h * 43) >> 3, noct = (((h + 1) * 43) >> 3) - oct0;
  const __amdgpu_buffer_rsrc_t rws = rsrc(ws, WS_END);
  const int qh = QSPREAD ? (bid & 6) * 4 + j : h, qc = QSPREAD ? (bid >> 6) & 1 : j & 1, qr = QSPREAD ? (bid & 1) * 2 + (bid >> 7) : j >> 1;
  v4u W[16], xa, xb;
  unsigned E;
  if (w >= 8) {
    const __amdgpu_buffer_rsrc_t rx = rsrc(x, 2 * DM * 2);
    xa = __builtin_amdgcn_raw_buffer_load_b128(rx, lane * 16, 0, 0);
    xb = __builtin_amdgcn_raw_buffer_load_b128(rx, lane * 16 + 1024, 0, 0);
    E = __builtin_amdgcn_raw_buffer_load_b32(rws, WS_E, 0, AUX_SC1);
    const GAS unsigned char* wp;
    int rs;
    if (w == 8) { wp = (const GAS unsigned char*)wq + (size_t)(qr * 128 + g * 32 + (c >> 3) * 16) * 8192 + qh * 256 + qc * 128 + (c & 7) * 16; rs = 8192; }
    else { wp = (const GAS unsigned char*)(c < 8 ? wg : wu) + (size_t)(j * 64 + g * 16) * (FF * 2) + (oct0 + min(c & 7, noct - 1)) * 16; rs = FF * 2; __builtin_amdgcn_s_sleep(2); }
#pragma unroll
    for (int i = 0; i < 16; ++i) { W[i] = __builtin_nontemporal_load((const GAS v4u*)wp); wp += rs; SB(); }
  } else if (PREFETCH && w == 0) {
    unsigned long long pc;
    asm volatile("s_getpc_b64 %0" : "=s"(pc));
    E = *(const GAS unsigned*)((pc & ~127ull) + min((bid >> 3) * 3 + (lane & 3), CODE_LINES) * 128);
  }
  STAMP(1);
  BAR();
  const int mr = (c & 7) >> 1;

  if (__builtin_expect(w < 8, 1)) {
    const size_t kvo = ((size_t)((j >> 2) * NH + h) * SK + (SK - WIN) + (j & 3) * 256) * DH;
    const __amdgpu_buffer_rsrc_t rk = rsrc(kc + kvo, 65536), rv = rsrc(vc + kvo, 65536);
    const int ko = w * 1024 + lane * 16;
    v4u K[8], V[8], WO[2], WD;
#pragma unroll
    for (int i = 0; i < 8; ++i) { if (i >= 4) THR(); K[i] = ld_nt(rk, ko, i * 8192); }
    if (PREFETCH && w == 0) asm volatile("" :: "v"(E));
#pragma unroll
    for (int i = 0; i <= QKPOS; ++i) { THR(); V[i] = ld_nt(rv, ko, i * 8192); }
    const unsigned hm = (c & 1) ? 0xffff0000u : 0x0000ffffu;
    const v4u mk = {mr == 0 ? hm : 0u, mr == 1 ? hm : 0u, mr == 2 ? hm : 0u, mr == 3 ? hm : 0u};
    STAMP(2);
    BAR();
    STAMP(3);
    const v4u q = *(LAS v4u*)(L + L_Q + c * 16);
    const unsigned T = *(LAS unsigned*)(L + L_T);
    float s[8];
#pragma unroll
    for (int i = 0; i < 8; ++i) s[i] = dot2(K[i].w, q.w, dot2(K[i].z, q.z, dot2(K[i].y, q.y, dot2(K[i].x, q.x, 0.f))));
#pragma unroll
    for (int i = 0; i < 4; ++i) { const float a = s[i] + dppf<X8>(s[i]), b = s[i + 4] + dppf<X8>(s[i + 4]); s[i] = (c & 8) ? b : a; }
#pragma unroll
    for (int i = 0; i < 2; ++i) { const float a = s[i] + dppf<X7>(s[i]), b = s[i + 2] + dppf<X7>(s[i + 2]); s[i] = (c & 4) ? b : a; }
    { const float a = s[0] + dppf<X2>(s[0]), b = s[1] + dppf<X2>(s[1]); s[0] = (c & 2) ? b : a; }
    const float dotv = s[0] + dppf<X1>(s[0]);
    const float sc = rbf(rbf(dotv) * 0.08838834764831845f) * 1.4426950408889634f;
    float mx = fmaxf(sc, dppf<X2>(sc)); mx = fmaxf(mx, dppf<X7>(mx)); mx = fmaxf(mx, dppf<X8>(mx)); mx = xrow_max(mx);
    *(LAS float*)(L + L_M + w * 4) = mx;
    if (QKPOS < 7) { THR(); V[QKPOS < 7 ? QKPOS + 1 : 7] = ld_nt(rv, ko, (QKPOS + 1) * 8192); }
    BAR();
    const v4f m0 = *(LAS v4f*)(L + L_M), m1 = *(LAS v4f*)(L + L_M + 16);
    const float M = fmaxf(fmaxf(fmaxf(m0.x, m0.y), fmaxf(m0.z, m0.w)), fmaxf(fmaxf(m1.x, m1.y), fmaxf(m1.z, m1.w)));
    const float p = __builtin_amdgcn_exp2f(sc - M);
    float l = p + dppf<X2>(p); l += dppf<X7>(l); l += dppf<X8>(l); l = xrow_sum(l);
    *(LAS float*)(L + L_L + w * 4) = l;
    const unsigned pp = pk(p, p);
#pragma unroll
    for (int i = QKPOS + 2; i < 8; ++i) { THR(); V[i] = ld_nt(rv, ko, i * 8192); }
    {
      const __amdgpu_buffer_rsrc_t ro = rsrc(wo + (size_t)h * DH * DM + j * 64, (DH * DM - j * 64) * 2);
      const int oo = (w * 16 + g * 4 + (c >> 3) * 2) * 1024 + (c & 7) * 16;
      const __amdgpu_buffer_rsrc_t rd = rsrc(wd + (size_t)oct0 * 8 * DM + j * 64, noct * 8192 - j * 128);
      THR(); WD = ld_nt(rd, (w * 8 + g * 2 + (c >> 3)) * 1024 + (c & 7) * 16, 0);
      THR(); WO[0] = ld_nt(ro, oo, 0);
      THR(); WO[1] = ld_nt(ro, oo + 1024, 0);
      SB();
    }
    STAMP(4);
    v4f o = {0.f, 0.f, 0.f, 0.f};
    o = mfma(sel(dppu<BC + 0>(pp), mk), V[0], o);
    o = mfma(sel(dppu<BC + 2>(pp), mk), V[1], o);
    o = mfma(sel(dppu<BC + 4>(pp), mk), V[2], o);
    o = mfma(sel(dppu<BC + 6>(pp), mk), V[3], o);
    o = mfma(sel(dppu<BC + 8>(pp), mk), V[4], o);
    o = mfma(sel(dppu<BC + 10>(pp), mk), V[5], o);
    o = mfma(sel(dppu<BC + 12>(pp), mk), V[6], o);
    o = mfma(sel(dppu<BC + 14>(pp), mk), V[7], o);
    if (g < 2) *(LAS v4f*)(L + L_RED + w * 512 + c * 32 + g * 16) = o;
    STAMP(5);
    BAR();
    if (w == 0) {
      const v4f l0 = *(LAS v4f*)(L + L_L), l1 = *(LAS v4f*)(L + L_L + 16);
      v4f r_[8];
#pragma unroll
      for (int u = 0; u < 8; ++u) r_[u] = *(LAS v4f*)(L + L_RED + u * 512 + (lane & 31) * 16);
      SB();
      v4f a = ((r_[0] + r_[1]) + (r_[2] + r_[3])) + ((r_[4] + r_[5]) + (r_[6] + r_[7]));
      const float Ls = ((l0.x + l0.y) + (l0.z + l0.w)) + ((l1.x + l1.y) + (l1.z + l1.w));
      const float e = M + __builtin_amdgcn_logf(Ls), eh = F(__float_as_uint(e) & ~TM);
      a *= __builtin_amdgcn_rcpf(Ls);
      if (lane == 32) a = v4f{eh, e - eh, 0.f, 0.f};
      if (lane <= 32) st_unit(rws, lane * 16, WS_O + (grp + j) * O_REC, a, T);
    }
    STAMP(6);
    v4f y0, y1;
    {
      const int hi = L_H + (w * 8 + g * 2 + (c >> 3)) * 2;
      const unsigned h0 = *(LAS u16*)(L + hi), h1 = *(LAS u16*)(L + hi + 128);
      const v4f z = {0.f, 0.f, 0.f, 0.f};
      y0 = mfma(sel(h0 | (h0 << 16), mk), WD, z);
      y1 = mfma(sel(h1 | (h1 << 16), mk), WD, z);
      asm volatile("" : "+v"(y0), "+v"(y1));
    }
    {
      const int go = (w * 8 + g * 2 + (c >> 3)) * 8, eo = (lane & 7) * O_REC + 512 + (lane & 8);
      v2u P[8], PE;
      unsigned sp_ = 0;
      for (;; ++sp_) {
        asm volatile("" ::: "memory");
        PE = ld_dev2(rws, eo, WS_O + grp * O_REC);
#pragma unroll
        for (int u = 0; u < 8; ++u) P[u] = ld_dev2(rws, go, WS_O + (grp + u) * O_REC);
        unsigned nb = bad(PE, T);
#pragma unroll
        for (int u = 0; u < 8; ++u) nb |= bad(P[u], T);
        if (all_ok(nb) || sp_ > SPIN_CAP) break;
      }
      STAMP(7);
      CNT(12, sp_);
      const float e = un(PE.x) + un(PE.y);
      float em = fmaxf(e, dppf<X1>(e)); em = fmaxf(em, dppf<X2>(em));
      float wt = __builtin_amdgcn_exp2f(e - em);
      float ws_ = wt + dppf<X1>(wt); ws_ += dppf<X2>(ws_);
      wt *= __builtin_amdgcn_rcpf(ws_);
      const float w0 = dppf<BC + 0>(wt), w1 = dppf<BC + 1>(wt), w2 = dppf<BC + 2>(wt), w3 = dppf<BC + 3>(wt);
      const float w4 = dppf<BC + 4>(wt), w5 = dppf<BC + 5>(wt), w6 = dppf<BC + 6>(wt), w7 = dppf<BC + 7>(wt);
      const float a0 = fmaf(w3, un(P[3].x), fmaf(w2, un(P[2].x), fmaf(w1, un(P[1].x), w0 * un(P[0].x))));
      const float a1 = fmaf(w3, un(P[3].y), fmaf(w2, un(P[2].y), fmaf(w1, un(P[1].y), w0 * un(P[0].y))));
      const float b0 = fmaf(w7, un(P[7].x), fmaf(w6, un(P[6].x), fmaf(w5, un(P[5].x), w4 * un(P[4].x))));
      const float b1 = fmaf(w7, un(P[7].y), fmaf(w6, un(P[6].y), fmaf(w5, un(P[5].y), w4 * un(P[4].y))));
      y0 = mfma(sel(pk(a0, a0), mk), WO[0], y0);
      y1 = mfma(sel(pk(b0, b0), mk), WO[0], y1);
      y0 = mfma(sel(pk(a1, a1), mk), WO[1], y0);
      y1 = mfma(sel(pk(b1, b1), mk), WO[1], y1);
    }
    if ((g >> 1) == (c >> 3)) {
      const int ro = L_RED2 + (w * 2 + (g >> 1)) * 512 + (c & 7) * 32 + (g & 1) * 16;
      *(LAS v4f*)(L + ro) = y0;
      *(LAS v4f*)(L + ro + 256) = y1;
    }
    STAMP(8);
    BAR();
    if (w != 0) { DUMP(); return; }
    {
      v4f r_[8];
#pragma unroll
      for (int u = 0; u < 8; ++u) r_[u] = *(LAS v4f*)(L + L_RED2 + u * 1024 + lane * 16);
      SB();
      v4f a = ((r_[0] + r_[1]) + (r_[2] + r_[3])) + ((r_[4] + r_[5]) + (r_[6] + r_[7]));
      {
        auto t0 = __builtin_amdgcn_permlane32_swap(__float_as_uint(a.x), __float_as_uint(a.x), false, false); a.x = F(t0[0]) + F(t0[1]);
        auto t1 = __builtin_amdgcn_permlane32_swap(__float_as_uint(a.y), __float_as_uint(a.y), false, false); a.y = F(t1[0]) + F(t1[1]);
        auto t2 = __builtin_amdgcn_permlane32_swap(__float_as_uint(a.z), __float_as_uint(a.z), false, false); a.z = F(t2[0]) + F(t2[1]);
        auto t3 = __builtin_amdgcn_permlane32_swap(__float_as_uint(a.w), __float_as_uint(a.w), false, false); a.w = F(t3[0]) + F(t3[1]);
      }
      if (lane < 32) st_unit(rws, lane * 16 + h * 512, WS_Y + j * 16384, a, T);
      STAMP(9);
      const int oi = (h >> 4) * DM + j * 64 + (h & 15) * 4 + (lane & 1) * 2;
      const unsigned xr = *(const GAS unsigned*)(x + oi);
      const int go = (lane >> 1) * 512 + h * 16 + (lane & 1) * 8;
      v2u P;
      unsigned sp_ = 0;
      for (;; ++sp_) {
        asm volatile("" ::: "memory");
        P = ld_dev2(rws, go, WS_Y + j * 16384);
        if (all_ok(bad(P, T)) || sp_ > SPIN_CAP) break;
      }
      STAMP(10);
      CNT(13, sp_);
      float u0 = un(P.x), u1 = un(P.y);
      u0 += dppf<X2>(u0); u1 += dppf<X2>(u1);
      u0 += dppf<R4>(u0); u1 += dppf<R4>(u1);
      u0 += dppf<X8>(u0); u1 += dppf<X8>(u1);
      u0 = xrow_sum(u0); u1 = xrow_sum(u1);
      if (lane < 2) *(GAS unsigned*)(out + oi) = pk(bflo(xr) + u0, bfhi(xr) + u1);
      if (bid == 0 && lane == 0) __builtin_amdgcn_raw_buffer_store_b32(T, rws, WS_E, 0, AUX_SC1);
      STAMP(11);
      DUMP();
    }
    return;
  }

  __builtin_amdgcn_s_setprio(3);
  asm volatile("" :: "v"(xa), "v"(xb));
  STAMP(5);
#pragma unroll
  for (int b = 0; b < 2; ++b) {
    const v4u v = b ? xb : xa;
    float f[8] = {bflo(v.x), bfhi(v.x), bflo(v.y), bfhi(v.y), bflo(v.z), bfhi(v.z), bflo(v.w), bfhi(v.w)};
    const float mu = wave_sum(((f[0] + f[1]) + (f[2] + f[3])) + ((f[4] + f[5]) + (f[6] + f[7]))) * (1.f / DM);
    float q = 0.f;
#pragma unroll
    for (int e = 0; e < 8; ++e) q = fmaf(f[e] - mu, f[e] - mu, q);
    const float var = wave_sum(q) * (1.f / DM);
    const float mean = rbf(mu), den = rbf(__builtin_amdgcn_sqrtf(rbf(rbf(var) + 1e-5f))), rd = __builtin_amdgcn_rcpf(den);
#pragma unroll
    for (int e = 0; e < 8; ++e) { const float n = rbf(f[e] - mean), t = n * rd; f[e] = fmaf(fmaf(-t, den, n), rd, t); }
    *(LAS v4u*)(L + L_X + b * 1024 + lane * 16) = v4u{pk(f[0], f[1]), pk(f[2], f[3]), pk(f[4], f[5]), pk(f[6], f[7])};
  }
  asm volatile("s_waitcnt lgkmcnt(0)" ::: "memory");
  STAMP(4);
  const unsigned T = (E + 1u) & TM;
  const unsigned z4 = 0x0c0c0c0cu, slo = (c & 1) ? 0x01000c0cu : 0x0c0c0100u, shi = (c & 1) ? 0x03020c0cu : 0x0c0c0302u;
  const v4u sl = {mr == 0 ? slo : z4, mr == 1 ? slo : z4, mr == 2 ? slo : z4, mr == 3 ? slo : z4};
  const v4u sh = {mr == 0 ? shi : z4, mr == 1 ? shi : z4, mr == 2 ? shi : z4, mr == 3 ? shi : z4};
  unsigned sp_ = 0;
  if (w == 8) {
    const int xo = L_X + qr * 256 + g * 64 + (c >> 3) * 32;
    const v4u a0 = *(LAS v4u*)(L + xo), a1 = *(LAS v4u*)(L + xo + 16), b0 = *(LAS v4u*)(L + xo + 1024), b1 = *(LAS v4u*)(L + xo + 1040);
    const unsigned xs0[8] = {a0.x, a0.y, a0.z, a0.w, a1.x, a1.y, a1.z, a1.w}, xs1[8] = {b0.x, b0.y, b0.z, b0.w, b1.x, b1.y, b1.z, b1.w};
    v4f d0 = {0.f, 0.f, 0.f, 0.f}, d1 = d0;
#pragma unroll
    for (int i = 0; i < 16; ++i) {
      d0 = mfma(selp(xs0[i >> 1], (i & 1) ? sh : sl), W[i], d0);
      d1 = mfma(selp(xs1[i >> 1], (i & 1) ? sh : sl), W[i], d1);
    }
    v4f r;
#pragma unroll
    for (int e = 0; e < 4; ++e) {
      auto t = __builtin_amdgcn_permlane32_swap(__float_as_uint(d0[e]), __float_as_uint(d1[e]), false, false);
      const float keep = lane < 32 ? F(t[0]) : F(t[1]), other = lane < 32 ? F(t[1]) : F(t[0]);
      r[e] = keep + dppf<X8>(other);
    }
    STAMP(2);
    if ((g >> 1) == (c >> 3)) st_unit(rws, (g >> 1) * 512 + (c & 7) * 32 + (g & 1) * 16, WS_Q + qh * 4096 + qr * 1024 + qc * 256, r, T);
    __builtin_amdgcn_s_sleep(QDELAY);
    STAMP(6);
    const int go = (lane >> 5) * 1024 + (lane & 31) * 16, so = WS_Q + h * 4096 + (j >> 2) * 512;
    v4u P0, P1;
    for (;; ++sp_) {
      asm volatile("" ::: "memory");
      P0 = ld_dev(rws, go, so);
      P1 = ld_dev(rws, go + 2048, so);
      if (all_ok(bad(P0, T) | bad(P1, T)) || sp_ > SPIN_CAP) break;
    }
    float qv[4];
#pragma unroll
    for (int e = 0; e < 4; ++e) {
      const float a = un(P0[e]) + un(P1[e]);
      auto t = __builtin_amdgcn_permlane32_swap(__float_as_uint(a), __float_as_uint(a), false, false);
      qv[e] = F(t[0]) + F(t[1]);
    }
    *(LAS v2u*)(L + L_Q + (lane & 31) * 8) = v2u{pk(qv[0], qv[1]), pk(qv[2], qv[3])};
    *(LAS unsigned*)(L + L_T) = T;
  } else {
    const int xo = L_X + (c >> 3) * 1024 + j * 128 + g * 32;
    const v4u a0 = *(LAS v4u*)(L + xo), a1 = *(LAS v4u*)(L + xo + 16);
    const unsigned xs[8] = {a0.x, a0.y, a0.z, a0.w, a1.x, a1.y, a1.z, a1.w};
    v4f d = {0.f, 0.f, 0.f, 0.f};
#pragma unroll
    for (int i = 0; i < 16; ++i) d = mfma(selp(xs[i >> 1], (i & 1) ? sh : sl), W[i], d);
    STAMP(2);
    const int fo = (g * 12 + (c >> 3) * 6 + min(c & 7, 5)) * 16;
    if ((c & 7) < 6) st_unit(rws, fo, WS_F + (grp + j) * 768, d, T);
    __builtin_amdgcn_s_sleep(FDELAY);
    STAMP(6);
    v4u P[8];
#pragma unroll
    for (int s = 0; s < 8; ++s) P[s] = ld_dev(rws, fo, WS_F + (grp + s) * 768);
    BAR();
    BAR();
    for (;; ++sp_) {
      unsigned nb = 0;
#pragma unroll
      for (int s = 0; s < 8; ++s) nb |= bad(P[s], T);
      if (all_ok(nb) || sp_ > SPIN_CAP) break;
      asm volatile("" ::: "memory");
#pragma unroll
      for (int s = 0; s < 8; ++s) P[s] = ld_dev(rws, fo, WS_F + (grp + s) * 768);
    }
    float hv[4];
#pragma unroll
    for (int r = 0; r < 4; ++r) {
      float a = 0.f;
#pragma unroll
      for (int s = 0; s < 8; ++s) a += un(P[s][r]);
      const float gt = rbf(a), up = dppf<X8>(gt);
      const float z = 0.7978845608028654f * (gt + 0.044715f * gt * gt * gt);
      const float ge = gt * __builtin_amdgcn_rcpf(1.f + __builtin_amdgcn_exp2f(-2.885390081777927f * z));
      hv[r] = rbf(ge) * up;
    }
    if (c < 8) *(LAS v2u*)(L + L_H + (g >> 1) * 128 + c * 16 + (g & 1) * 8) = c < noct ? v2u{pk(hv[0], hv[1]), pk(hv[2], hv[3])} : v2u{0u, 0u};
  }
  STAMP(3);
  CNT(12, sp_);
  DUMP();
}

__global__ void tiny(unsigned* p) { if (p == nullptr) __builtin_trap(); }
__global__ void __launch_bounds__(640) fat(unsigned* p) { __shared__ unsigned l[3744]; l[threadIdx.x] = 1; asm volatile("v_mov_b32 v147, 0" ::: "v147"); if (p == nullptr) p[0] = l[threadIdx.x + 1]; }
__global__ void __launch_bounds__(640) wide(unsigned* p) { if (p == nullptr) __builtin_trap(); }
extern "C" void kernel_launch(void* const* d_in, const int* in_sizes, int n_in, void* d_out, int out_size, void* d_ws, size_t ws_size,
                              hipStream_t stream) {
  fused_decode<<<256, 640, 0, stream>>>((const u16*)d_in[0], (const u16*)d_in[1], (const u16*)d_in[2], (const u16*)d_in[3], (const u16*)d_in[4],
                                        (const u16*)d_in[6], (const u16*)d_in[7], (const u16*)d_in[8], (u16*)d_out, (unsigned char*)d_ws);
  fat<<<256, 640, 0, stream>>>((unsigned*)d_ws);
}
```

```cpp
#include <hip/hip_runtime.h>
#include <stdint.h>

__global__ void __launch_bounds__(640, 3) fused_decode(const void* x, const void* kc, const void* vc, const void* wq, const void* wo, const void* wg,
                                                    const void* wu, const void* wd, void* out, void* ws) {
  __shared__ __attribute__((aligned(16))) unsigned char lds[32768];
  asm volatile(
    ".Lstart_fd:\n"
    "s_load_dwordx16 s[4:19], s[0:1], 0x0\n"
    "s_load_dwordx4 s[20:23], s[0:1], 0x40\n"
    "v_readfirstlane_b32 s24, v0\n"
    "v_and_b32 v1, 63, v0\n"
    "s_and_b32 s25, s2, 7\n"
    "s_lshl_b32 s25, s25, 2\n"
    "s_lshr_b32 s26, s2, 6\n"
    "s_add_u32 s25, s25, s26\n"
    "s_bfe_u32 s26, s2, 0x30003\n"
    "s_lshr_b32 s24, s24, 6\n"
    "s_sub_u32 s24, s24, 2\n"
    "s_cselect_b32 s3, 10, 0\n"
    "s_add_u32 s24, s24, s3\n"
    "v_lshlrev_b32 v2, 4, v1\n"
    "v_lshrrev_b32 v4, 3, v1\n"
    "v_and_b32 v7, 0x70, v2\n"
    "s_mov_b32 s31, 0x20000\n"
    "s_mov_b32 s42, 0xd3400\n"
    "s_mov_b32 s43, s31\n"
    "s_cmp_lt_u32 s24, 8\n"
    "s_cbranch_scc1 .Lstream_fd\n"
    "v_lshlrev_b32 v60, 5, v1\n"
    "v_lshl_or_b32 v61, v4, 17, v7\n"
    "s_mov_b32 s56, s25\n"
    "s_lshr_b32 s57, s26, 1\n"
    "s_and_b32 s58, s26, 1\n"
    "s_lshl_b32 s44, s57, 20\n"
    "s_lshl_b32 s45, s56, 8\n"
    "s_or_b32 s44, s44, s45\n"
    "s_lshl_b32 s45, s58, 7\n"
    "s_or_b32 s44, s44, s45\n"
    "s_mov_b32 s30, 0x400000\n"
    "s_waitcnt lgkmcnt(0)\n"
    "global_load_dwordx4 v[4:7], v60, s[4:5]\n"
    "global_load_dwordx4 v[8:11], v60, s[4:5] offset:16\n"
    "s_mov_b32 s40, s22\n"
    "s_and_b32 s41, s23, 0xffff\n"
    "s_cmp_eq_u32 s24, 8\n"
    "buffer_load_dword v3, off, s[40:43], 0 sc1\n"
    "s_cbranch_scc0 .Lauxbar_fd\n"
    "s_mov_b32 s28, s10\n"
    "s_and_b32 s29, s11, 0xffff\n"
    "buffer_load_dwordx4 v[64:67], v61, s[28:31], s44 offen nt\n"
    "s_addk_i32 s44, 0x2000\n"
    "buffer_load_dwordx4 v[68:71], v61, s[28:31], s44 offen nt\n"
    "s_addk_i32 s44, 0x2000\n"
    "buffer_load_dwordx4 v[72:75], v61, s[28:31], s44 offen nt\n"
    "s_addk_i32 s44, 0x2000\n"
    "buffer_load_dwordx4 v[76:79], v61, s[28:31], s44 offen nt\n"
    "s_addk_i32 s44, 0x2000\n"
    "buffer_load_dwordx4 v[80:83], v61, s[28:31], s44 offen nt\n"
    "s_addk_i32 s44, 0x2000\n"
    "buffer_load_dwordx4 v[84:87], v61, s[28:31], s44 offen nt\n"
    "s_addk_i32 s44, 0x2000\n"
    "buffer_load_dwordx4 v[88:91], v61, s[28:31], s44 offen nt\n"
    "s_addk_i32 s44, 0x2000\n"
    "buffer_load_dwordx4 v[92:95], v61, s[28:31], s44 offen nt\n"
    "s_addk_i32 s44, 0x2000\n"
    "buffer_load_dwordx4 v[96:99], v61, s[28:31], s44 offen nt\n"
    "s_addk_i32 s44, 0x2000\n"
    "buffer_load_dwordx4 v[100:103], v61, s[28:31], s44 offen nt\n"
    "s_addk_i32 s44, 0x2000\n"
    "buffer_load_dwordx4 v[104:107], v61, s[28:31], s44 offen nt\n"
    "s_addk_i32 s44, 0x2000\n"
    "buffer_load_dwordx4 v[108:111], v61, s[28:31], s44 offen nt\n"
    "s_addk_i32 s44, 0x2000\n"
    "buffer_load_dwordx4 v[112:115], v61, s[28:31], s44 offen nt\n"
    "s_addk_i32 s44, 0x2000\n"
    "buffer_load_dwordx4 v[116:119], v61, s[28:31], s44 offen nt\n"
    "s_addk_i32 s44, 0x2000\n"
    "buffer_load_dwordx4 v[120:123], v61, s[28:31], s44 offen nt\n"
    "s_addk_i32 s44, 0x2000\n"
    "buffer_load_dwordx4 v[124:127], v61, s[28:31], s44 offen nt\n"
    "s_barrier\n"
    "s_barrier\n"
    "s_barrier\n"
    "s_branch .Laux_fd\n"
    ".Lauxbar_fd:\n"
    "s_barrier\n"
    "s_barrier\n"
    "s_barrier\n"
    "s_branch .Laux_fd\n"
    ".Lstream_fd:\n"
    "s_cmp_eq_u32 s24, 1\n"
    "s_cbranch_scc0 .Lnp_fd\n"
    "s_getpc_b64 s[46:47]\n"
    ".Lpc_fd:\n"
    "s_lshr_b32 s45, s2, 3\n"
    "s_mul_i32 s45, s45, 384\n"
    "v_and_b32 v109, 3, v1\n"
    "v_lshl_add_u32 v109, v109, 7, s45\n"
    "v_min_u32 v109, (.Lend_fd-.Lstart_fd-4), v109\n"
    "s_sub_u32 s46, s46, (.Lpc_fd-.Lstart_fd)\n"
    "s_subb_u32 s47, s47, 0\n"
    "global_load_dword v108, v109, s[46:47]\n"
    ".Lnp_fd:\n"
    "s_lshr_b32 s44, s26, 2\n"
    "s_lshl_b32 s44, s44, 5\n"
    "s_add_u32 s44, s44, s25\n"
    "s_lshl_b32 s44, s44, 20\n"
    "s_and_b32 s45, s26, 3\n"
    "s_lshl_b32 s45, s45, 16\n"
    "s_or_b32 s44, s44, s45\n"
    "s_or_b32 s44, s44, 0xc0000\n"
    "s_and_b32 s45, s24, 1\n"
    "s_lshl_b32 s45, s45, 4\n"
    "s_and_b32 s46, s24, 2\n"
    "s_or_b32 s45, s45, s46\n"
    "s_lshr_b32 s46, s24, 2\n"
    "s_lshl_b32 s46, s46, 3\n"
    "s_or_b32 s45, s45, s46\n"
    "s_mul_i32 s47, s2, 11\n"
    "s_and_b32 s46, s47, 0x1a\n"
    "s_xor_b32 s45, s45, s46\n"
    "s_lshl_b32 s45, s45, 10\n"
    "v_or_b32 v3, s45, v2\n"
    "v_lshl_or_b32 v4, v4, 10, v7\n"
    "s_lshl_b32 s45, s24, 3\n"
    "v_mov_b32 v6, s45\n"
    "v_mov_b32 v8, 0\n"
    "v_mov_b32 v9, -1.0\n"
    "v_mov_b32 v10, 0\n"
    "v_mov_b32 v11, 0\n"
    "ds_write_b64 v6, v[8:9] offset:576\n"
    "v_mov_b32 v12, 0\n"
    "v_mov_b32 v13, 0\n"
    "ds_write_b128 v8, v[10:13] offset:512\n"
    "s_mov_b32 s30, 0x10000\n"
    "s_mov_b32 s34, s30\n"
    "s_mov_b32 s35, s31\n"
    "s_and_b32 s47, s47, 5\n"
    "s_and_b32 s48, s2, 32\n"
    "s_or_b32 s48, s48, s47\n"
    "s_lshl_b32 s48, s48, 10\n"
    "s_xor_b32 s49, s48, 0x400\n"
    "s_xor_b32 s50, s48, 0x1000\n"
    "s_xor_b32 s51, s48, 0x1400\n"
    "s_xor_b32 s52, s48, 0x8000\n"
    "s_xor_b32 s53, s48, 0x8400\n"
    "s_xor_b32 s54, s48, 0x9000\n"
    "s_xor_b32 s55, s48, 0x9400\n"
    "v_bfe_u32 v6, v1, 4, 1\n"
    "v_lshlrev_b32 v6, 9, v6\n"
    "v_and_or_b32 v6, v1, 15, v6\n"
    "s_lshl_b32 s45, s26, 2\n"
    "s_and_b32 s46, s24, 3\n"
    "s_or_b32 s45, s45, s46\n"
    "s_lshl_b32 s45, s45, 4\n"
    "v_add_lshl_u32 v6, v6, s45, 1\n"
    "v_mov_b32 v5, 0\n"
    "s_waitcnt lgkmcnt(0)\n"
    "s_add_u32 s28, s6, s44\n"
    "s_addc_u32 s29, s7, 0\n"
    "s_and_b32 s29, s29, 0xffff\n"
    "s_barrier\n"
    "buffer_load_dwordx4 v[8:11], v3, s[28:31], s48 offen nt\n"
    "s_add_u32 s32, s8, s44\n"
    "s_addc_u32 s33, s9, 0\n"
    "s_and_b32 s33, s33, 0xffff\n"
    "s_bitcmp1_b32 s24, 2\n"
    "s_cbranch_scc0 .Lnx_fd\n"
    "global_load_short_d16_hi v5, v6, s[4:5]\n"
    ".Lnx_fd:\n"
    "s_barrier\n"
    "buffer_load_dwordx4 v[12:15], v3, s[28:31], s49 offen nt\n"
    "s_barrier\n"
    "buffer_load_dwordx4 v[16:19], v3, s[28:31], s50 offen nt\n"
    "s_memrealtime s[62:63]\n"
    "s_waitcnt lgkmcnt(0)\n"
    "s_mov_b32 s64, s62\n"
    "s_mov_b32 s40, s22\n"
    "s_and_b32 s41, s23, 0xffff\n"
    "s_lshr_b32 s27, s25, 2\n"
    "s_cmp_lt_u32 s27, 3\n"
    "s_cselect_b32 s3, 6, 5\n"
    "s_min_u32 s44, s27, 3\n"
    "s_mul_i32 s27, s27, 5\n"
    "s_add_u32 s27, s27, s44\n"
    "s_lshl_b32 s27, s27, 2\n"
    "s_and_b32 s44, s25, 3\n"
    "s_mul_i32 s44, s44, s3\n"
    "s_add_u32 s27, s27, s44\n"
    "s_lshl_b32 s44, s27, 13\n"
    "s_lshl_b32 s45, s26, 7\n"
    "s_add_u32 s44, s44, s45\n"
    "s_add_u32 s36, s18, s44\n"
    "s_addc_u32 s37, s19, 0\n"
    "s_and_b32 s37, s37, 0xffff\n"
    "s_lshl_b32 s38, s3, 13\n"
    "s_sub_u32 s38, s38, s45\n"
    "s_mov_b32 s39, s31\n"
    "s_lshl_b32 s57, s24, 13\n"
    "s_lshl_b32 s59, s24, 14\n"
    "s_lshl_b32 s56, s24, 10\n"
    "s_lshl_b32 s58, s24, 11\n"
    "s_add_u32 s56, s56, 24576\n"
    "s_add_u32 s58, s58, 8192\n"
    "v_and_b32 v84, 1, v1\n"
    "v_lshlrev_b32 v84, 4, v84\n"
    "v_mov_b32 v85, 0xffff\n"
    "v_lshlrev_b32 v84, v84, v85\n"
    "v_bfe_u32 v85, v1, 1, 2\n"
    "v_lshlrev_b32 v7, 1, v1\n"
    "v_and_b32 v7, 32, v7\n"
    "v_xor_b32 v4, v4, v7\n"
    "v_xor_b32 v7, 64, v4\n"
    "s_and_b32 s44, s24, 1\n"
    "s_lshl_b32 s44, s44, 6\n"
    "v_xor_b32 v110, s44, v4\n"
    "v_cmp_eq_u32 vcc, 0, v85\n"
    "s_nop 1\n"
    "v_cndmask_b32 v88, 0, v84, vcc\n"
    "v_cmp_eq_u32 vcc, 1, v85\n"
    "s_nop 1\n"
    "v_cndmask_b32 v89, 0, v84, vcc\n"
    "v_cmp_eq_u32 vcc, 2, v85\n"
    "s_nop 1\n"
    "v_cndmask_b32 v90, 0, v84, vcc\n"
    "v_cmp_eq_u32 vcc, 3, v85\n"
    "s_nop 1\n"
    "v_cndmask_b32 v91, 0, v84, vcc\n"
    "v_and_b32 v94, 15, v1\n"
    "v_lshlrev_b32 v94, 5, v94\n"
    "v_and_b32 v84, 16, v1\n"
    "s_lshl_b32 s44, s24, 9\n"
    "v_or3_b32 v94, v94, v84, s44\n"
    "v_mov_b32 v95, 0\n"
    "s_lshl_b32 s44, s24, 3\n"
    "v_mov_b32 v87, s44\n"
    "v_and_b32 v84, 2, v1\n"
    "v_cmp_ne_u32 s[60:61], 0, v84\n"
    "s_waitcnt vmcnt(2)\n"
    "buffer_load_dwordx4 v[20:23], v3, s[28:31], s51 offen nt\n"
    "s_memrealtime s[62:63]\n"
    "s_waitcnt lgkmcnt(0)\n"
    "s_sub_u32 s62, s62, s64\n"
    "s_cmp_gt_u32 s62, 169\n"
    "s_cbranch_scc1 .Lad4_fd\n"
    "s_waitcnt vmcnt(1)\n"
    ".Lad4_fd:\n"
    "s_waitcnt vmcnt(2)\n"
    "buffer_load_dwordx4 v[24:27], v3, s[28:31], s52 offen nt\n"
    "s_memrealtime s[62:63]\n"
    "s_waitcnt lgkmcnt(0)\n"
    "s_sub_u32 s62, s62, s64\n"
    "s_cmp_gt_u32 s62, 197\n"
    "s_cbranch_scc1 .Lad5_fd\n"
    "s_waitcnt vmcnt(1)\n"
    ".Lad5_fd:\n"
    "s_waitcnt vmcnt(2)\n"
    "buffer_load_dwordx4 v[28:31], v3, s[28:31], s53 offen nt\n"
    "s_memrealtime s[62:63]\n"
    "s_waitcnt lgkmcnt(0)\n"
    "s_sub_u32 s62, s62, s64\n"
    "s_cmp_gt_u32 s62, 225\n"
    "s_cbranch_scc1 .Lad6_fd\n"
    "s_waitcnt vmcnt(1)\n"
    ".Lad6_fd:\n"
    "s_waitcnt vmcnt(2)\n"
    "buffer_load_dwordx4 v[32:35], v3, s[28:31], s54 offen nt\n"
    "s_memrealtime s[62:63]\n"
    "s_waitcnt lgkmcnt(0)\n"
    "s_sub_u32 s62, s62, s64\n"
    "s_cmp_gt_u32 s62, 253\n"
    "s_cbranch_scc1 .Lad7_fd\n"
    "s_waitcnt vmcnt(1)\n"
    ".Lad7_fd:\n"
    "s_waitcnt vmcnt(2)\n"
    "buffer_load_dwordx4 v[36:39], v3, s[28:31], s55 offen nt\n"
    "s_lshl_b32 s44, s25, 17\n"
    "s_lshl_b32 s45, s26, 7\n"
    "s_add_u32 s44, s44, s45\n"
    "s_add_u32 s28, s12, s44\n"
    "s_addc_u32 s29, s13, 0\n"
    "s_and_b32 s29, s29, 0xffff\n"
    "s_sub_u32 s30, 0x20000, s45\n"
    "s_memrealtime s[62:63]\n"
    "s_waitcnt lgkmcnt(0)\n"
    "s_sub_u32 s62, s62, s64\n"
    "s_cmp_gt_u32 s62, 281\n"
    "s_cbranch_scc1 .Lad8_fd\n"
    "s_waitcnt vmcnt(1)\n"
    ".Lad8_fd:\n"
    "s_waitcnt vmcnt(2)\n"
    "buffer_load_dwordx4 v[40:43], v3, s[32:35], s48 offen nt\n"
    "s_memrealtime s[62:63]\n"
    "s_waitcnt lgkmcnt(0)\n"
    "s_sub_u32 s62, s62, s64\n"
    "s_cmp_gt_u32 s62, 309\n"
    "s_cbranch_scc1 .Lad9_fd\n"
    "s_waitcnt vmcnt(1)\n"
    ".Lad9_fd:\n"
    "s_waitcnt vmcnt(2)\n"
    "buffer_load_dwordx4 v[44:47], v3, s[32:35], s49 offen nt\n"
    "s_memrealtime s[62:63]\n"
    "s_waitcnt lgkmcnt(0)\n"
    "s_sub_u32 s62, s62, s64\n"
    "s_cmp_gt_u32 s62, 337\n"
    "s_cbranch_scc1 .Lad10_fd\n"
    "s_waitcnt vmcnt(1)\n"
    ".Lad10_fd:\n"
    "s_waitcnt vmcnt(2)\n"
    "buffer_load_dwordx4 v[48:51], v3, s[32:35], s50 offen nt\n"
    "s_memrealtime s[62:63]\n"
    "s_waitcnt lgkmcnt(0)\n"
    "s_sub_u32 s62, s62, s64\n"
    "s_cmp_gt_u32 s62, 365\n"
    "s_cbranch_scc1 .Lad11_fd\n"
    "s_waitcnt vmcnt(1)\n"
    ".Lad11_fd:\n"
    "s_waitcnt vmcnt(2)\n"
    "buffer_load_dwordx4 v[52:55], v3, s[32:35], s51 offen nt\n"
    "s_memrealtime s[62:63]\n"
    "s_waitcnt lgkmcnt(0)\n"
    "s_sub_u32 s62, s62, s64\n"
    "s_cmp_gt_u32 s62, 393\n"
    "s_cbranch_scc1 .Lad12_fd\n"
    "s_waitcnt vmcnt(1)\n"
    ".Lad12_fd:\n"
    "s_waitcnt vmcnt(2)\n"
    "buffer_load_dwordx4 v[56:59], v3, s[32:35], s52 offen nt\n"
    "s_memrealtime s[62:63]\n"
    "s_waitcnt lgkmcnt(0)\n"
    "s_sub_u32 s62, s62, s64\n"
    "s_cmp_gt_u32 s62, 421\n"
    "s_cbranch_scc1 .Lad13_fd\n"
    "s_waitcnt vmcnt(1)\n"
    ".Lad13_fd:\n"
    "s_waitcnt vmcnt(2)\n"
    "buffer_load_dwordx4 v[60:63], v3, s[32:35], s53 offen nt\n"
    "v_and_b32 v84, 0xf0, v2\n"
    ".Lqspin_fd:\n"
    "ds_read_b32 v85, v95 offset:512\n"
    "ds_read_b128 v[72:75], v84 offset:0\n"
    "s_waitcnt lgkmcnt(0)\n"
    "v_readfirstlane_b32 s47, v85\n"
    "s_nop 0\n"
    "s_cmp_eq_u32 s47, 0\n"
    "s_cbranch_scc1 .Lqspin_fd\n"
    "s_waitcnt vmcnt(6)\n"
    "v_dot2_f32_bf16 v76, v8, v72, 0\n"
    "v_dot2_f32_bf16 v77, v12, v72, 0\n"
    "v_dot2_f32_bf16 v78, v16, v72, 0\n"
    "v_dot2_f32_bf16 v79, v20, v72, 0\n"
    "v_dot2_f32_bf16 v80, v24, v72, 0\n"
    "v_dot2_f32_bf16 v81, v28, v72, 0\n"
    "v_dot2_f32_bf16 v82, v32, v72, 0\n"
    "v_dot2_f32_bf16 v83, v36, v72, 0\n"
    "v_dot2c_f32_bf16 v76, v9, v73\n"
    "v_dot2c_f32_bf16 v77, v13, v73\n"
    "v_dot2c_f32_bf16 v78, v17, v73\n"
    "v_dot2c_f32_bf16 v79, v21, v73\n"
    "v_dot2c_f32_bf16 v80, v25, v73\n"
    "v_dot2c_f32_bf16 v81, v29, v73\n"
    "v_dot2c_f32_bf16 v82, v33, v73\n"
    "v_dot2c_f32_bf16 v83, v37, v73\n"
    "v_dot2c_f32_bf16 v76, v10, v74\n"
    "v_dot2c_f32_bf16 v77, v14, v74\n"
    "v_dot2c_f32_bf16 v78, v18, v74\n"
    "v_dot2c_f32_bf16 v79, v22, v74\n"
    "v_dot2c_f32_bf16 v80, v26, v74\n"
    "v_dot2c_f32_bf16 v81, v30, v74\n"
    "v_dot2c_f32_bf16 v82, v34, v74\n"
    "v_dot2c_f32_bf16 v83, v38, v74\n"
    "v_dot2c_f32_bf16 v76, v11, v75\n"
    "v_dot2c_f32_bf16 v77, v15, v75\n"
    "v_dot2c_f32_bf16 v78, v19, v75\n"
    "v_dot2c_f32_bf16 v79, v23, v75\n"
    "v_dot2c_f32_bf16 v80, v27, v75\n"
    "v_dot2c_f32_bf16 v81, v31, v75\n"
    "v_dot2c_f32_bf16 v82, v35, v75\n"
    "v_dot2c_f32_bf16 v83, v39, v75\n"
    "s_and_b32 s47, s47, 0x3ff\n"
    "s_memrealtime s[62:63]\n"
    "s_waitcnt lgkmcnt(0)\n"
    "s_sub_u32 s62, s62, s64\n"
    "s_cmp_gt_u32 s62, 449\n"
    "s_cbranch_scc1 .Lad14_fd\n"
    "s_waitcnt vmcnt(1)\n"
    ".Lad14_fd:\n"
    "s_waitcnt vmcnt(2)\n"
    "buffer_load_dwordx4 v[64:67], v3, s[32:35], s54 offen nt\n"
    "v_add_f32_dpp v76, v76, v76 row_ror:8 row_mask:0xf bank_mask:0x3\n"
    "v_add_f32_dpp v77, v77, v77 row_ror:8 row_mask:0xf bank_mask:0x3\n"
    "v_add_f32_dpp v78, v78, v78 row_ror:8 row_mask:0xf bank_mask:0x3\n"
    "v_add_f32_dpp v79, v79, v79 row_ror:8 row_mask:0xf bank_mask:0x3\n"
    "v_add_f32_dpp v76, v80, v80 row_ror:8 row_mask:0xf bank_mask:0xc\n"
    "v_add_f32_dpp v77, v81, v81 row_ror:8 row_mask:0xf bank_mask:0xc\n"
    "v_add_f32_dpp v78, v82, v82 row_ror:8 row_mask:0xf bank_mask:0xc\n"
    "v_add_f32_dpp v79, v83, v83 row_ror:8 row_mask:0xf bank_mask:0xc\n"
    "v_add_f32_dpp v76, v76, v76 row_half_mirror row_mask:0xf bank_mask:0x5\n"
    "v_add_f32_dpp v77, v77, v77 row_half_mirror row_mask:0xf bank_mask:0x5\n"
    "v_add_f32_dpp v76, v78, v78 row_half_mirror row_mask:0xf bank_mask:0xa\n"
    "v_add_f32_dpp v77, v79, v79 row_half_mirror row_mask:0xf bank_mask:0xa\n"
    "s_nop 0\n"
    "v_add_f32_dpp v76, v76, v76 quad_perm:[2,3,0,1] row_mask:0xf bank_mask:0xf bound_ctrl:1\n"
    "v_add_f32_dpp v77, v77, v77 quad_perm:[2,3,0,1] row_mask:0xf bank_mask:0xf bound_ctrl:1\n"
    "v_cndmask_b32 v76, v76, v77, s[60:61]\n"
    "s_nop 1\n"
    "v_add_f32_dpp v76, v76, v76 quad_perm:[1,0,3,2] row_mask:0xf bank_mask:0xf bound_ctrl:1\n"
    "v_cvt_pk_bf16_f32 v76, v76, v76\n"
    "v_lshlrev_b32 v76, 16, v76\n"
    "v_mul_f32 v76, 0x3db504f3, v76\n"
    "v_cvt_pk_bf16_f32 v76, v76, v76\n"
    "v_lshlrev_b32 v76, 16, v76\n"
    "v_mul_f32 v93, 0x3fb8aa3b, v76\n"
    "s_nop 1\n"
    "v_max_f32_dpp v78, v93, v93 quad_perm:[2,3,0,1] row_mask:0xf bank_mask:0xf bound_ctrl:1\n"
    "s_nop 1\n"
    "v_max_f32_dpp v78, v78, v78 row_half_mirror row_mask:0xf bank_mask:0xf bound_ctrl:1\n"
    "s_nop 1\n"
    "v_max_f32_dpp v78, v78, v78 row_ror:8 row_mask:0xf bank_mask:0xf bound_ctrl:1\n"
    "v_mov_b32 v77, v78\n"
    "s_nop 1\n"
    "v_permlane16_swap_b32 v78, v77\n"
    "v_max_f32 v78, v78, v77\n"
    "v_mov_b32 v77, v78\n"
    "s_nop 1\n"
    "v_permlane32_swap_b32 v78, v77\n"
    "v_max_f32 v78, v78, v77\n"
    "v_sub_f32 v76, v93, v78\n"
    "v_exp_f32 v76, v76\n"
    "s_nop 1\n"
    "v_add_f32_dpp v79, v76, v76 quad_perm:[2,3,0,1] row_mask:0xf bank_mask:0xf bound_ctrl:1\n"
    "v_cvt_pk_bf16_f32 v92, v76, v76\n"
    "s_nop 0\n"
    "v_add_f32_dpp v79, v79, v79 row_half_mirror row_mask:0xf bank_mask:0xf bound_ctrl:1\n"
    "s_nop 1\n"
    "v_add_f32_dpp v79, v79, v79 row_ror:8 row_mask:0xf bank_mask:0xf bound_ctrl:1\n"
    "v_mov_b32 v77, v79\n"
    "s_nop 1\n"
    "v_permlane16_swap_b32 v79, v77\n"
    "v_add_f32 v79, v79, v77\n"
    "v_mov_b32 v77, v79\n"
    "s_nop 1\n"
    "v_permlane32_swap_b32 v79, v77\n"
    "v_add_f32 v79, v79, v77\n"
    "ds_write_b64 v87, v[78:79] offset:576\n"
    "s_memrealtime s[62:63]\n"
    "s_waitcnt lgkmcnt(0)\n"
    "s_sub_u32 s62, s62, s64\n"
    "s_cmp_gt_u32 s62, 477\n"
    "s_cbranch_scc1 .Lad15_fd\n"
    "s_waitcnt vmcnt(1)\n"
    ".Lad15_fd:\n"
    "s_waitcnt vmcnt(2)\n"
    "buffer_load_dwordx4 v[68:71], v3, s[32:35], s55 offen nt\n"
    "v_mov_b32 v86, 1\n"
    "s_mov_b64 exec, 1\n"
    "ds_add_u32 v95, v86 offset:520\n"
    "s_mov_b64 exec, -1\n"
    "s_cmp_eq_u32 s24, 0\n"
    "s_cbranch_scc0 .Lnostat_fd\n"
    ".Lmlspin_fd:\n"
    "ds_read_b128 v[8:11], v95 offset:576\n"
    "ds_read_b128 v[12:15], v95 offset:592\n"
    "ds_read_b128 v[16:19], v95 offset:608\n"
    "ds_read_b128 v[20:23], v95 offset:624\n"
    "s_waitcnt lgkmcnt(0)\n"
    "v_min3_f32 v36, v9, v11, v13\n"
    "v_min3_f32 v37, v15, v17, v19\n"
    "v_min3_f32 v36, v36, v21, v23\n"
    "v_min_f32 v36, v36, v37\n"
    "v_cmp_gt_f32 vcc, 0, v36\n"
    "s_cbranch_vccnz .Lmlspin_fd\n"
    "v_max3_f32 v36, v8, v10, v12\n"
    "v_max3_f32 v37, v14, v16, v18\n"
    "v_max3_f32 v36, v36, v20, v22\n"
    "v_max_f32 v36, v36, v37\n"
    "v_sub_f32 v24, v8, v36\n"
    "v_sub_f32 v25, v10, v36\n"
    "v_sub_f32 v26, v12, v36\n"
    "v_sub_f32 v27, v14, v36\n"
    "v_sub_f32 v28, v16, v36\n"
    "v_sub_f32 v29, v18, v36\n"
    "v_sub_f32 v30, v20, v36\n"
    "v_sub_f32 v31, v22, v36\n"
    "v_exp_f32 v24, v24\n"
    "v_exp_f32 v25, v25\n"
    "v_exp_f32 v26, v26\n"
    "v_exp_f32 v27, v27\n"
    "v_exp_f32 v28, v28\n"
    "v_exp_f32 v29, v29\n"
    "v_exp_f32 v30, v30\n"
    "v_exp_f32 v31, v31\n"
    "v_mul_f32 v37, v24, v9\n"
    "v_fmac_f32 v37, v25, v11\n"
    "v_fmac_f32 v37, v26, v13\n"
    "v_fmac_f32 v37, v27, v15\n"
    "v_fmac_f32 v37, v28, v17\n"
    "v_fmac_f32 v37, v29, v19\n"
    "v_fmac_f32 v37, v30, v21\n"
    "v_fmac_f32 v37, v31, v23\n"
    "v_log_f32 v39, v37\n"
    "v_rcp_f32 v38, v37\n"
    "v_mov_b32 v37, s47\n"
    "v_add_f32 v36, v36, v39\n"
    "v_pk_mul_f32 v[24:25], v[24:25], v[38:39] op_sel_hi:[1,0]\n"
    "v_pk_mul_f32 v[26:27], v[26:27], v[38:39] op_sel_hi:[1,0]\n"
    "v_pk_mul_f32 v[28:29], v[28:29], v[38:39] op_sel_hi:[1,0]\n"
    "v_pk_mul_f32 v[30:31], v[30:31], v[38:39] op_sel_hi:[1,0]\n"
    "s_mov_b32 s44, 0xc0c0400\n"
    "v_perm_b32 v32, v36, v37, s44\n"
    "s_mov_b32 s44, 0xc0c0500\n"
    "v_perm_b32 v33, v36, v37, s44\n"
    "s_mov_b32 s44, 0xc0c0600\n"
    "v_perm_b32 v34, v36, v37, s44\n"
    "s_mov_b32 s44, 0xc0c0700\n"
    "v_perm_b32 v35, v36, v37, s44\n"
    "s_lshl_b32 s44, s25, 3\n"
    "s_add_u32 s44, s44, s26\n"
    "s_lshl_b32 s44, s44, 9\n"
    "s_add_u32 s44, s44, 0x71000\n"
    "s_mov_b32 s45, 0xffff0000\n"
    ".Lnostat_fd:\n"
    "v_and_b32_dpp v96, v92, v88 row_newbcast:0 row_mask:0xf bank_mask:0xf bound_ctrl:1\n"
    "v_and_b32_dpp v97, v92, v89 row_newbcast:0 row_mask:0xf bank_mask:0xf bound_ctrl:1\n"
    "v_and_b32_dpp v98, v92, v90 row_newbcast:0 row_mask:0xf bank_mask:0xf bound_ctrl:1\n"
    "v_and_b32_dpp v99, v92, v91 row_newbcast:0 row_mask:0xf bank_mask:0xf bound_ctrl:1\n"
    "s_waitcnt vmcnt(7)\n"
    "s_nop 0\n"
    "v_mfma_f32_16x16x32_bf16 v[104:107], v[96:99], v[40:43], 0\n"
    "v_and_b32_dpp v100, v92, v88 row_newbcast:2 row_mask:0xf bank_mask:0xf bound_ctrl:1\n"
    "v_and_b32_dpp v101, v92, v89 row_newbcast:2 row_mask:0xf bank_mask:0xf bound_ctrl:1\n"
    "v_and_b32_dpp v102, v92, v90 row_newbcast:2 row_mask:0xf bank_mask:0xf bound_ctrl:1\n"
    "v_and_b32_dpp v103, v92, v91 row_newbcast:2 row_mask:0xf bank_mask:0xf bound_ctrl:1\n"
    "s_waitcnt vmcnt(6)\n"
    "s_nop 0\n"
    "v_mfma_f32_16x16x32_bf16 v[104:107], v[100:103], v[44:47], v[104:107]\n"
    "v_and_b32_dpp v96, v92, v88 row_newbcast:4 row_mask:0xf bank_mask:0xf bound_ctrl:1\n"
    "v_and_b32_dpp v97, v92, v89 row_newbcast:4 row_mask:0xf bank_mask:0xf bound_ctrl:1\n"
    "v_and_b32_dpp v98, v92, v90 row_newbcast:4 row_mask:0xf bank_mask:0xf bound_ctrl:1\n"
    "v_and_b32_dpp v99, v92, v91 row_newbcast:4 row_mask:0xf bank_mask:0xf bound_ctrl:1\n"
    "s_waitcnt vmcnt(5)\n"
    "s_nop 0\n"
    "v_mfma_f32_16x16x32_bf16 v[104:107], v[96:99], v[48:51], v[104:107]\n"
    "v_and_b32_dpp v100, v92, v88 row_newbcast:6 row_mask:0xf bank_mask:0xf bound_ctrl:1\n"
    "v_and_b32_dpp v101, v92, v89 row_newbcast:6 row_mask:0xf bank_mask:0xf bound_ctrl:1\n"
    "v_and_b32_dpp v102, v92, v90 row_newbcast:6 row_mask:0xf bank_mask:0xf bound_ctrl:1\n"
    "v_and_b32_dpp v103, v92, v91 row_newbcast:6 row_mask:0xf bank_mask:0xf bound_ctrl:1\n"
    "s_waitcnt vmcnt(4)\n"
    "s_nop 0\n"
    "v_mfma_f32_16x16x32_bf16 v[104:107], v[100:103], v[52:55], v[104:107]\n"
    "v_and_b32_dpp v96, v92, v88 row_newbcast:8 row_mask:0xf bank_mask:0xf bound_ctrl:1\n"
    "v_and_b32_dpp v97, v92, v89 row_newbcast:8 row_mask:0xf bank_mask:0xf bound_ctrl:1\n"
    "v_and_b32_dpp v98, v92, v90 row_newbcast:8 row_mask:0xf bank_mask:0xf bound_ctrl:1\n"
    "v_and_b32_dpp v99, v92, v91 row_newbcast:8 row_mask:0xf bank_mask:0xf bound_ctrl:1\n"
    "s_waitcnt vmcnt(3)\n"
    "s_nop 0\n"
    "v_mfma_f32_16x16x32_bf16 v[104:107], v[96:99], v[56:59], v[104:107]\n"
    "v_and_b32_dpp v100, v92, v88 row_newbcast:10 row_mask:0xf bank_mask:0xf bound_ctrl:1\n"
    "v_and_b32_dpp v101, v92, v89 row_newbcast:10 row_mask:0xf bank_mask:0xf bound_ctrl:1\n"
    "v_and_b32_dpp v102, v92, v90 row_newbcast:10 row_mask:0xf bank_mask:0xf bound_ctrl:1\n"
    "v_and_b32_dpp v103, v92, v91 row_newbcast:10 row_mask:0xf bank_mask:0xf bound_ctrl:1\n"
    "s_waitcnt vmcnt(2)\n"
    "s_nop 0\n"
    "v_mfma_f32_16x16x32_bf16 v[104:107], v[100:103], v[60:63], v[104:107]\n"
    "s_mov_b32 m0, s56\n"
    "s_nop 0\n"
    "buffer_load_dwordx4 v110, s[36:39], s57 offen nt lds\n"
    "v_and_b32_dpp v96, v92, v88 row_newbcast:12 row_mask:0xf bank_mask:0xf bound_ctrl:1\n"
    "v_and_b32_dpp v97, v92, v89 row_newbcast:12 row_mask:0xf bank_mask:0xf bound_ctrl:1\n"
    "v_and_b32_dpp v98, v92, v90 row_newbcast:12 row_mask:0xf bank_mask:0xf bound_ctrl:1\n"
    "v_and_b32_dpp v99, v92, v91 row_newbcast:12 row_mask:0xf bank_mask:0xf bound_ctrl:1\n"
    "s_waitcnt vmcnt(2)\n"
    "s_nop 0\n"
    "v_mfma_f32_16x16x32_bf16 v[104:107], v[96:99], v[64:67], v[104:107]\n"
    "v_and_b32_dpp v100, v92, v88 row_newbcast:14 row_mask:0xf bank_mask:0xf bound_ctrl:1\n"
    "v_and_b32_dpp v101, v92, v89 row_newbcast:14 row_mask:0xf bank_mask:0xf bound_ctrl:1\n"
    "v_and_b32_dpp v102, v92, v90 row_newbcast:14 row_mask:0xf bank_mask:0xf bound_ctrl:1\n"
    "v_and_b32_dpp v103, v92, v91 row_newbcast:14 row_mask:0xf bank_mask:0xf bound_ctrl:1\n"
    "s_waitcnt vmcnt(1)\n"
    "s_nop 0\n"
    "v_mfma_f32_16x16x32_bf16 v[104:107], v[100:103], v[68:71], v[104:107]\n"
    "s_mov_b32 m0, s58\n"
    "s_nop 0\n"
    "buffer_load_dwordx4 v4, s[28:31], s59 offen nt lds\n"
    "s_xor_b32 m0, s58, 0x400\n"
    "s_xor_b32 s59, s59, 0x2000\n"
    "buffer_load_dwordx4 v7, s[28:31], s59 offen nt lds\n"
    "s_mov_b32 exec_hi, 0\n"
    "s_cmp_eq_u32 s24, 0\n"
    "ds_write_b128 v94, v[104:107] offset:3328\n"
    "s_waitcnt lgkmcnt(0)\n"
    "s_barrier\n"
    "s_cbranch_scc0 .Lnot0_fd\n"
    "ds_read_b128 v[40:43], v2 offset:3328\n"
    "ds_read_b128 v[44:47], v2 offset:3840\n"
    "ds_read_b128 v[48:51], v2 offset:4352\n"
    "ds_read_b128 v[52:55], v2 offset:4864\n"
    "ds_read_b128 v[56:59], v2 offset:5376\n"
    "ds_read_b128 v[60:63], v2 offset:5888\n"
    "ds_read_b128 v[64:67], v2 offset:6400\n"
    "ds_read_b128 v[68:71], v2 offset:6912\n"
    "s_waitcnt lgkmcnt(7)\n"
    "v_pk_mul_f32 v[72:73], v[40:41], v[24:25] op_sel_hi:[1,0]\n"
    "v_pk_mul_f32 v[74:75], v[42:43], v[24:25] op_sel_hi:[1,0]\n"
    "s_waitcnt lgkmcnt(5)\n"
    "v_pk_fma_f32 v[72:73], v[44:45], v[24:25], v[72:73] op_sel:[0,1,0] op_sel_hi:[1,1,1]\n"
    "v_pk_fma_f32 v[74:75], v[46:47], v[24:25], v[74:75] op_sel:[0,1,0] op_sel_hi:[1,1,1]\n"
    "v_pk_fma_f32 v[72:73], v[48:49], v[26:27], v[72:73] op_sel_hi:[1,0,1]\n"
    "v_pk_fma_f32 v[74:75], v[50:51], v[26:27], v[74:75] op_sel_hi:[1,0,1]\n"
    "s_waitcnt lgkmcnt(3)\n"
    "v_pk_fma_f32 v[72:73], v[52:53], v[26:27], v[72:73] op_sel:[0,1,0] op_sel_hi:[1,1,1]\n"
    "v_pk_fma_f32 v[74:75], v[54:55], v[26:27], v[74:75] op_sel:[0,1,0] op_sel_hi:[1,1,1]\n"
    "v_pk_fma_f32 v[72:73], v[56:57], v[28:29], v[72:73] op_sel_hi:[1,0,1]\n"
    "v_pk_fma_f32 v[74:75], v[58:59], v[28:29], v[74:75] op_sel_hi:[1,0,1]\n"
    "s_waitcnt lgkmcnt(0)\n"
    "v_pk_fma_f32 v[72:73], v[60:61], v[28:29], v[72:73] op_sel:[0,1,0] op_sel_hi:[1,1,1]\n"
    "v_pk_fma_f32 v[74:75], v[62:63], v[28:29], v[74:75] op_sel:[0,1,0] op_sel_hi:[1,1,1]\n"
    "v_pk_fma_f32 v[72:73], v[64:65], v[30:31], v[72:73] op_sel_hi:[1,0,1]\n"
    "v_pk_fma_f32 v[74:75], v[66:67], v[30:31], v[74:75] op_sel_hi:[1,0,1]\n"
    "v_pk_fma_f32 v[72:73], v[68:69], v[30:31], v[72:73] op_sel:[0,1,0] op_sel_hi:[1,1,1]\n"
    "v_pk_fma_f32 v[74:75], v[70:71], v[30:31], v[74:75] op_sel:[0,1,0] op_sel_hi:[1,1,1]\n"
    "v_cvt_pk_bf16_f32 v72, v72, v73\n"
    "v_cvt_pk_bf16_f32 v73, v74, v75\n"
    "v_lshl_or_b32 v76, v72, 16, v32\n"
    "v_and_or_b32 v77, v72, s45, v33\n"
    "v_lshl_or_b32 v78, v73, 16, v34\n"
    "v_and_or_b32 v79, v73, s45, v35\n"
    "buffer_store_dwordx4 v[76:79], v2, s[40:43], s44 offen sc1\n"
    "v_mov_b32 v8, 1\n"
    "s_mov_b32 exec_lo, 1\n"
    "s_waitcnt vmcnt(1)\n"
    "ds_add_u32 v95, v8 offset:516\n"
    "s_endpgm\n"
    ".Lnot0_fd:\n"
    "s_mov_b32 exec_hi, -1\n"
    "v_mov_b32 v8, 1\n"
    "s_bitcmp1_b32 s24, 2\n"
    "s_cbranch_scc1 .Lcons_fd\n"
    "s_cmp_eq_u32 s24, 2\n"
    "s_cbranch_scc1 .Lfinal_fd\n"
    "s_setprio 2\n"
    "s_lshr_b32 s45, s24, 1\n"
    "s_lshl_b32 s44, s25, 12\n"
    "s_lshl_b32 s46, s45, 11\n"
    "s_add_u32 s44, s44, s46\n"
    "s_add_u32 s44, s44, 0x71000\n"
    "s_mul_i32 s45, s45, 320\n"
    "v_and_b32 v30, 31, v1\n"
    "v_lshlrev_b32 v30, 3, v30\n"
    "v_lshrrev_b32 v31, 5, v1\n"
    "v_lshl_or_b32 v30, v31, 2, v30\n"
    "v_add_u32 v30, s45, v30\n"
    "s_and_b32 s49, s47, 0xff\n"
    "s_mul_i32 s49, s49, 0x1010101\n"
    "v_mov_b32 v29, s49\n"
    "s_mov_b32 s49, 0x06040200\n"
    "s_mov_b32 s45, 0x07050301\n"
    "s_mov_b32 s46, 0x05040100\n"
    "s_mov_b32 s48, 0\n"
    "s_sleep 11\n"
    "buffer_load_dwordx4 v[12:15], v2, s[40:43], s44 offen sc1\n"
    "buffer_load_dwordx4 v[16:19], v2, s[40:43], s44 offen offset:1024 sc1\n"
    "s_mov_b64 exec, 1\n"
    "s_waitcnt vmcnt(2)\n"
    "ds_add_u32 v95, v8 offset:516\n"
    "s_mov_b64 exec, -1\n"
    "s_branch .Lo_chk_fd\n"
    ".Lo_fd:\n"
    "buffer_load_dwordx4 v[12:15], v2, s[40:43], s44 offen sc1\n"
    "buffer_load_dwordx4 v[16:19], v2, s[40:43], s44 offen offset:1024 sc1\n"
    ".Lo_chk_fd:\n"
    "s_waitcnt vmcnt(1)\n"
    "v_perm_b32 v21, v13, v12, s46\n"
    "v_perm_b32 v22, v15, v14, s46\n"
    "v_perm_b32 v20, v22, v21, s49\n"
    "v_perm_b32 v21, v22, v21, s45\n"
    "s_waitcnt vmcnt(0)\n"
    "v_perm_b32 v23, v17, v16, s46\n"
    "v_perm_b32 v24, v19, v18, s46\n"
    "v_xor_b32 v20, v20, v29\n"
    "v_perm_b32 v25, v24, v23, s49\n"
    "v_perm_b32 v23, v24, v23, s45\n"
    "v_bitop3_b32 v20, v20, v25, v29 bitop3:0xf6\n"
    "v_max_f32 v22, v21, v23\n"
    "v_cmp_ne_u32 vcc, 0, v20\n"
    "v_mov_b32 v24, v22\n"
    "s_cbranch_vccz .Lo_ok_fd\n"
    "s_add_u32 s48, s48, 1\n"
    "s_cmp_lt_u32 s48, 0x10000\n"
    "s_cbranch_scc1 .Lo_fd\n"
    ".Lo_ok_fd:\n"
    "v_and_b32 v12, 0xffff0000, v12\n"
    "v_and_b32 v13, 0xffff0000, v13\n"
    "v_permlane32_swap_b32 v22, v24\n"
    "v_and_b32 v14, 0xffff0000, v14\n"
    "v_max_f32 v22, v22, v24\n"
    "v_and_b32 v15, 0xffff0000, v15\n"
    "v_and_b32 v16, 0xffff0000, v16\n"
    "v_and_b32 v17, 0xffff0000, v17\n"
    "v_sub_f32 v21, v21, v22\n"
    "v_sub_f32 v23, v23, v22\n"
    "v_exp_f32 v20, v21\n"
    "v_exp_f32 v22, v23\n"
    "v_and_b32 v18, 0xffff0000, v18\n"
    "v_and_b32 v19, 0xffff0000, v19\n"
    "v_add_f32 v24, v20, v22\n"
    "v_pk_mul_f32 v[12:13], v[12:13], v[20:21] op_sel_hi:[1,0]\n"
    "v_mov_b32 v25, v24\n"
    "v_pk_mul_f32 v[14:15], v[14:15], v[20:21] op_sel_hi:[1,0]\n"
    "v_pk_fma_f32 v[12:13], v[16:17], v[22:23], v[12:13] op_sel_hi:[1,0,1]\n"
    "v_permlane32_swap_b32 v24, v25\n"
    "v_pk_fma_f32 v[14:15], v[18:19], v[22:23], v[14:15] op_sel_hi:[1,0,1]\n"
    "v_add_f32 v24, v24, v25\n"
    "v_rcp_f32 v24, v24\n"
    "v_permlane32_swap_b32 v12, v14\n"
    "v_permlane32_swap_b32 v13, v15\n"
    "v_pk_add_f32 v[12:13], v[12:13], v[14:15]\n"
    "v_pk_mul_f32 v[12:13], v[12:13], v[24:25] op_sel_hi:[1,0]\n"
    "v_cvt_pk_bf16_f32 v12, v12, v13\n"
    "ds_write_b32 v30, v12 offset:640\n"
    "s_waitcnt lgkmcnt(0)\n"
    "s_barrier\n"
    "s_endpgm\n"
    ".Lcons_fd:\n"
    "s_and_b32 s45, s24, 3\n"
    "v_lshrrev_b32 v56, 4, v1\n"
    "v_lshlrev_b32 v57, 10, v56\n"
    "v_bfe_u32 v58, v1, 2, 2\n"
    "v_lshl_or_b32 v57, v58, 7, v57\n"
    "v_and_b32 v58, 3, v1\n"
    "v_lshl_or_b32 v57, v58, 3, v57\n"
    "v_bfe_u32 v58, v1, 3, 2\n"
    "v_xor_b32 v58, s45, v58\n"
    "v_lshl_or_b32 v57, v58, 5, v57\n"
    "v_bfe_u32 v58, v1, 2, 1\n"
    "v_lshlrev_b32 v56, 4, v56\n"
    "v_lshl_or_b32 v59, v58, 7, v56\n"
    "v_mul_u32_u24 v58, 320, v58\n"
    "v_add_u32 v58, v58, v56\n"
    "s_lshl_b32 s46, s45, 8\n"
    "v_lshl_add_u32 v56, v1, 3, s46\n"
    "s_lshl_b32 s44, s26, 10\n"
    "s_add_u32 s44, s44, 0xd1000\n"
    "s_add_u32 s56, s22, s44\n"
    "s_addc_u32 s57, s23, 0\n"
    "s_or_b32 s49, s45, s26\n"
    "s_mov_b32 s45, 0xfffff000\n"
    "s_mov_b32 s52, 0x47800000\n"
    "s_mov_b32 s53, 0x39000000\n"
    "s_mul_i32 s54, s47, 0x400801\n"
    "s_mov_b64 exec, 1\n"
    "s_waitcnt vmcnt(0)\n"
    "ds_add_u32 v95, v8 offset:516\n"
    "s_mov_b64 exec, -1\n"
    ".Lcspin_fd:\n"
    "ds_read_b32 v9, v95 offset:516\n"
    "s_waitcnt lgkmcnt(0)\n"
    "v_readfirstlane_b32 s46, v9\n"
    "s_nop 0\n"
    "s_cmp_eq_u32 s46, 9\n"
    "s_cbranch_scc0 .Lcspin_fd\n"
    "ds_read_b128 v[32:35], v59 offset:256\n"
    "ds_read_b128 v[36:39], v59 offset:320\n"
    "ds_read_b64_tr_b16 v[24:25], v57 offset:24576\n"
    "ds_read_b64_tr_b16 v[26:27], v57 offset:25088\n"
    "ds_read_b64_tr_b16 v[28:29], v57 offset:28672\n"
    "ds_read_b64_tr_b16 v[30:31], v57 offset:29184\n"
    "ds_read_b64_tr_b16 v[8:9], v57 offset:8192\n"
    "ds_read_b64_tr_b16 v[10:11], v57 offset:8704\n"
    "ds_read_b64_tr_b16 v[12:13], v57 offset:12288\n"
    "ds_read_b64_tr_b16 v[14:15], v57 offset:12800\n"
    "ds_read_b64_tr_b16 v[16:17], v57 offset:16384\n"
    "ds_read_b64_tr_b16 v[18:19], v57 offset:16896\n"
    "ds_read_b64_tr_b16 v[20:21], v57 offset:20480\n"
    "ds_read_b64_tr_b16 v[22:23], v57 offset:20992\n"
    "s_waitcnt lgkmcnt(8)\n"
    "v_mfma_f32_16x16x32_bf16 v[48:51], v[32:35], v[24:27], 0\n"
    "v_mfma_f32_16x16x32_bf16 v[48:51], v[36:39], v[28:31], v[48:51]\n"
    "s_waitcnt vmcnt(0) lgkmcnt(0)\n"
    "s_barrier\n"
    "ds_read_b128 v[32:35], v58 offset:640\n"
    "ds_read_b128 v[36:39], v58 offset:704\n"
    "ds_read_b128 v[40:43], v58 offset:768\n"
    "ds_read_b128 v[44:47], v58 offset:832\n"
    "s_waitcnt lgkmcnt(3)\n"
    "v_mfma_f32_16x16x32_bf16 v[48:51], v[32:35], v[8:11], v[48:51]\n"
    "s_waitcnt lgkmcnt(2)\n"
    "v_mfma_f32_16x16x32_bf16 v[48:51], v[36:39], v[12:15], v[48:51]\n"
    "s_waitcnt lgkmcnt(1)\n"
    "v_mfma_f32_16x16x32_bf16 v[48:51], v[40:43], v[16:19], v[48:51]\n"
    "s_waitcnt lgkmcnt(0)\n"
    "v_mfma_f32_16x16x32_bf16 v[48:51], v[44:47], v[20:23], v[48:51]\n"
    "s_mov_b32 exec_hi, 0\n"
    "s_nop 6\n"
    "v_mul_f32 v60, 0x44800000, v48\n"
    "v_cmp_nlt_f32 s[50:51], |v48|, s52\n"
    "v_fract_f32 v61, v60\n"
    "v_cmp_le_f32 vcc, s53, |v48|\n"
    "v_mul_f32 v61, 0x4f800000, v61\n"
    "v_cvt_flr_i32_f32 v53, v60\n"
    "v_cvt_u32_f32 v61, v61\n"
    "s_cmp_lg_u64 s[50:51], 0\n"
    "s_cbranch_scc1 .Lraw_fd\n"
    "v_and_or_b32 v52, v61, s45, 1\n"
    "s_cbranch_vccz .Lraw_fd\n"
    ".Ladd_fd:\n"
    "global_atomic_add_x2 v[54:55], v56, v[52:53], s[56:57] sc0\n"
    "s_waitcnt vmcnt(0)\n"
    "v_and_b32 v62, 63, v54\n"
    "v_add_co_u32 v54, vcc, v54, v52\n"
    "v_cmp_eq_u32 s[50:51], 31, v62\n"
    "s_nop 0\n"
    "v_addc_co_u32 v55, vcc, v55, v53, vcc\n"
    "v_lshrrev_b32 v60, 12, v54\n"
    "v_cvt_f32_i32 v55, v55\n"
    "v_cvt_f32_u32 v60, v60\n"
    "v_bfe_u32 v61, v54, 6, 6\n"
    "v_fmac_f32 v5, 0x3a800000, v55\n"
    "s_and_b64 exec, exec, s[50:51]\n"
    "s_cbranch_execz .Lfin_fd\n"
    "v_cmp_ne_u32 vcc, 0, v61\n"
    "v_fmac_f32 v5, 0x30800000, v60\n"
    "s_cbranch_vccnz .Lslow_fd\n"
    ".Lout_fd:\n"
    "v_cvt_pk_bf16_f32 v10, v5, v5\n"
    "global_store_short v6, v10, s[20:21]\n"
    "s_cmp_eq_u32 s49, 0\n"
    "s_cbranch_scc0 .Lfin_fd\n"
    "s_and_b64 exec, exec, 1\n"
    "s_cbranch_execz .Lfin_fd\n"
    "v_mov_b32 v29, s47\n"
    "buffer_store_dword v29, off, s[40:43], 0 sc1\n"
    ".Lfin_fd:\n"
    "s_endpgm\n"
    ".Lraw_fd:\n"
    "s_lshl_b32 s46, s26, 5\n"
    "s_add_u32 s46, s46, s25\n"
    "s_lshl_b32 s46, s46, 10\n"
    "s_add_u32 s46, s46, 0x91000\n"
    "v_mov_b32 v49, s54\n"
    "buffer_store_dwordx2 v[48:49], v56, s[40:43], s46 offen sc1\n"
    "v_mov_b32 v52, 65\n"
    "v_mov_b32 v53, 0\n"
    "s_branch .Ladd_fd\n"
    ".Lslow_fd:\n"
    "s_mov_b32 s48, 0\n"
    ".Lslow_pass_fd:\n"
    "s_lshl_b32 s46, s26, 15\n"
    "s_add_u32 s46, s46, 0x91000\n"
    "s_mov_b32 s55, 0\n"
    "v_mov_b32 v12, 0\n"
    "v_mov_b32 v13, 0\n"
    ".Lslow_src_fd:\n"
    "buffer_load_dwordx2 v[14:15], v56, s[40:43], s46 offen sc1\n"
    "s_waitcnt vmcnt(0)\n"
    "v_cmp_eq_u32 vcc, s54, v15\n"
    "s_nop 1\n"
    "v_cndmask_b32 v14, 0, v14, vcc\n"
    "v_addc_co_u32 v13, vcc, 0, v13, vcc\n"
    "v_add_f32 v12, v12, v14\n"
    "s_addk_i32 s46, 0x400\n"
    "s_add_u32 s55, s55, 1\n"
    "s_cmp_lt_u32 s55, 32\n"
    "s_cbranch_scc1 .Lslow_src_fd\n"
    "v_cmp_ne_u32 vcc, v13, v61\n"
    "s_cbranch_vccz .Lslow_ok_fd\n"
    "s_add_u32 s48, s48, 1\n"
    "s_cmp_lt_u32 s48, 0x400\n"
    "s_cbranch_scc1 .Lslow_pass_fd\n"
    ".Lslow_ok_fd:\n"
    "v_add_f32 v5, v5, v12\n"
    "s_branch .Lout_fd\n"
    ".Lfinal_fd:\n"
    "s_mov_b64 exec, 1\n"
    "s_waitcnt vmcnt(0)\n"
    "ds_add_u32 v95, v8 offset:516\n"
    "s_endpgm\n"
    ".Laux_fd:\n"
    "s_setprio 3\n"
    "v_and_b32 v62, 15, v1\n"
    "v_lshrrev_b32 v63, 4, v1\n"
    "s_cmp_eq_u32 s24, 8\n"
    "s_cbranch_scc1 .Lnf_fd\n"
    "s_lshr_b32 s27, s25, 2\n"
    "s_cmp_lt_u32 s27, 3\n"
    "s_cselect_b32 s3, 6, 5\n"
    "s_min_u32 s44, s27, 3\n"
    "s_mul_i32 s27, s27, 5\n"
    "s_add_u32 s27, s27, s44\n"
    "s_lshl_b32 s27, s27, 2\n"
    "s_and_b32 s44, s25, 3\n"
    "s_mul_i32 s44, s44, s3\n"
    "s_add_u32 s27, s27, s44\n"
    "v_and_b32 v56, 7, v1\n"
    "s_sub_u32 s44, s3, 1\n"
    "v_min_u32 v56, s44, v56\n"
    "v_add_lshl_u32 v56, v56, s27, 4\n"
    "s_lshl_b32 s44, s26, 6\n"
    "s_add_u32 s44, s44, 1\n"
    "v_lshl_add_u32 v57, v63, 4, s44\n"
    "s_movk_i32 s44, 0xac0\n"
    "v_mad_u32_u24 v56, v57, s44, v56\n"
    "v_mov_b32 v57, 0\n"
    "v_cmp_gt_u32 vcc, 8, v62\n"
    "v_mov_b32 v58, s16\n"
    "v_mov_b32 v59, s14\n"
    "v_cndmask_b32 v58, v58, v59, vcc\n"
    "v_mov_b32 v59, s17\n"
    "v_mov_b32 v61, s15\n"
    "v_cndmask_b32 v59, v59, v61, vcc\n"
    "v_lshl_add_u64 v[56:57], v[56:57], 0, v[58:59]\n"
    "s_movk_i32 s44, 0x2040\n"
    "s_mov_b32 s45, 0\n"
    "global_load_dwordx4 v[64:67], v[56:57], off offset:-2752 nt\n"
    "global_load_dwordx4 v[68:71], v[56:57], off nt\n"
    "global_load_dwordx4 v[72:75], v[56:57], off offset:2752 nt\n"
    "v_lshl_add_u64 v[56:57], v[56:57], 0, s[44:45]\n"
    "global_load_dwordx4 v[76:79], v[56:57], off offset:-2752 nt\n"
    "global_load_dwordx4 v[80:83], v[56:57], off nt\n"
    "global_load_dwordx4 v[84:87], v[56:57], off offset:2752 nt\n"
    "v_lshl_add_u64 v[56:57], v[56:57], 0, s[44:45]\n"
    "global_load_dwordx4 v[88:91], v[56:57], off offset:-2752 nt\n"
    "global_load_dwordx4 v[92:95], v[56:57], off nt\n"
    "global_load_dwordx4 v[96:99], v[56:57], off offset:2752 nt\n"
    "v_lshl_add_u64 v[56:57], v[56:57], 0, s[44:45]\n"
    "global_load_dwordx4 v[100:103], v[56:57], off offset:-2752 nt\n"
    "global_load_dwordx4 v[104:107], v[56:57], off nt\n"
    "global_load_dwordx4 v[108:111], v[56:57], off offset:2752 nt\n"
    "v_lshl_add_u64 v[56:57], v[56:57], 0, s[44:45]\n"
    "global_load_dwordx4 v[112:115], v[56:57], off offset:-2752 nt\n"
    "global_load_dwordx4 v[116:119], v[56:57], off nt\n"
    "global_load_dwordx4 v[120:123], v[56:57], off offset:2752 nt\n"
    "v_lshl_add_u64 v[56:57], v[56:57], 0, s[44:45]\n"
    "global_load_dwordx4 v[124:127], v[56:57], off offset:-2752 nt\n"
    ".Lnf_fd:\n"
    "v_bfe_u32 v56, v1, 1, 2\n"
    "v_and_b32 v57, 1, v1\n"
    "v_cmp_eq_u32 vcc, 1, v57\n"
    "v_mov_b32 v58, 0x0c0c0100\n"
    "v_mov_b32 v59, 0x01000c0c\n"
    "v_cndmask_b32 v58, v58, v59, vcc\n"
    "v_mov_b32 v59, 0x202\n"
    "v_mov_b32 v57, 0x02020000\n"
    "v_cndmask_b32 v59, v59, v57, vcc\n"
    "v_add_u32 v59, v58, v59\n"
    "v_mov_b32 v57, 0x0c0c0c0c\n"
    "v_cmp_eq_u32 vcc, 0, v56\n"
    "s_nop 1\n"
    "v_cndmask_b32 v48, v57, v58, vcc\n"
    "v_cndmask_b32 v52, v57, v59, vcc\n"
    "v_cmp_eq_u32 vcc, 1, v56\n"
    "s_nop 1\n"
    "v_cndmask_b32 v49, v57, v58, vcc\n"
    "v_cndmask_b32 v53, v57, v59, vcc\n"
    "v_cmp_eq_u32 vcc, 2, v56\n"
    "s_nop 1\n"
    "v_cndmask_b32 v50, v57, v58, vcc\n"
    "v_cndmask_b32 v54, v57, v59, vcc\n"
    "v_cmp_eq_u32 vcc, 3, v56\n"
    "s_nop 1\n"
    "v_cndmask_b32 v51, v57, v58, vcc\n"
    "v_cndmask_b32 v55, v57, v59, vcc\n"
    "s_waitcnt vmcnt(16)\n"
    "v_lshlrev_b32 v12, 16, v4\n"
    "v_and_b32 v13, 0xffff0000, v4\n"
    "v_lshlrev_b32 v14, 16, v5\n"
    "v_and_b32 v15, 0xffff0000, v5\n"
    "v_lshlrev_b32 v16, 16, v6\n"
    "v_and_b32 v17, 0xffff0000, v6\n"
    "v_lshlrev_b32 v18, 16, v7\n"
    "v_and_b32 v19, 0xffff0000, v7\n"
    "v_lshlrev_b32 v20, 16, v8\n"
    "v_and_b32 v21, 0xffff0000, v8\n"
    "v_lshlrev_b32 v22, 16, v9\n"
    "v_and_b32 v23, 0xffff0000, v9\n"
    "v_lshlrev_b32 v24, 16, v10\n"
    "v_and_b32 v25, 0xffff0000, v10\n"
    "v_lshlrev_b32 v26, 16, v11\n"
    "v_and_b32 v27, 0xffff0000, v11\n"
    "v_pk_add_f32 v[32:33], v[12:13], v[14:15]\n"
    "v_pk_add_f32 v[34:35], v[16:17], v[18:19]\n"
    "v_pk_add_f32 v[36:37], v[20:21], v[22:23]\n"
    "v_pk_add_f32 v[38:39], v[24:25], v[26:27]\n"
    "v_pk_add_f32 v[32:33], v[32:33], v[34:35]\n"
    "v_pk_add_f32 v[36:37], v[36:37], v[38:39]\n"
    "v_pk_add_f32 v[32:33], v[32:33], v[36:37]\n"
    "v_add_f32 v28, v32, v33\n"
    "s_nop 1\n"
    "v_add_f32_dpp v28, v28, v28 quad_perm:[1,0,3,2] row_mask:0xf bank_mask:0xf bound_ctrl:1\n"
    "s_nop 1\n"
    "v_add_f32_dpp v28, v28, v28 quad_perm:[2,3,0,1] row_mask:0xf bank_mask:0xf bound_ctrl:1\n"
    "s_nop 1\n"
    "v_add_f32_dpp v28, v28, v28 row_half_mirror row_mask:0xf bank_mask:0xf bound_ctrl:1\n"
    "s_nop 1\n"
    "v_add_f32_dpp v28, v28, v28 row_ror:8 row_mask:0xf bank_mask:0xf bound_ctrl:1\n"
    "v_mov_b32 v29, v28\n"
    "s_nop 1\n"
    "v_permlane16_swap_b32 v28, v29\n"
    "v_add_f32 v28, v28, v29\n"
    "v_mul_f32 v58, 0x3b000000, v28\n"
    "v_pk_add_f32 v[32:33], v[12:13], v[58:59] op_sel_hi:[1,0] neg_lo:[0,1] neg_hi:[0,1]\n"
    "v_pk_add_f32 v[34:35], v[14:15], v[58:59] op_sel_hi:[1,0] neg_lo:[0,1] neg_hi:[0,1]\n"
    "v_pk_add_f32 v[36:37], v[16:17], v[58:59] op_sel_hi:[1,0] neg_lo:[0,1] neg_hi:[0,1]\n"
    "v_pk_add_f32 v[38:39], v[18:19], v[58:59] op_sel_hi:[1,0] neg_lo:[0,1] neg_hi:[0,1]\n"
    "v_pk_add_f32 v[40:41], v[20:21], v[58:59] op_sel_hi:[1,0] neg_lo:[0,1] neg_hi:[0,1]\n"
    "v_pk_add_f32 v[42:43], v[22:23], v[58:59] op_sel_hi:[1,0] neg_lo:[0,1] neg_hi:[0,1]\n"
    "v_pk_add_f32 v[44:45], v[24:25], v[58:59] op_sel_hi:[1,0] neg_lo:[0,1] neg_hi:[0,1]\n"
    "v_pk_add_f32 v[46:47], v[26:27], v[58:59] op_sel_hi:[1,0] neg_lo:[0,1] neg_hi:[0,1]\n"
    "v_pk_mul_f32 v[28:29], v[32:33], v[32:33]\n"
    "v_pk_mul_f32 v[30:31], v[34:35], v[34:35]\n"
    "v_pk_fma_f32 v[28:29], v[36:37], v[36:37], v[28:29]\n"
    "v_pk_fma_f32 v[30:31], v[38:39], v[38:39], v[30:31]\n"
    "v_pk_fma_f32 v[28:29], v[40:41], v[40:41], v[28:29]\n"
    "v_pk_fma_f32 v[30:31], v[42:43], v[42:43], v[30:31]\n"
    "v_pk_fma_f32 v[28:29], v[44:45], v[44:45], v[28:29]\n"
    "v_pk_fma_f32 v[30:31], v[46:47], v[46:47], v[30:31]\n"
    "v_pk_add_f32 v[28:29], v[28:29], v[30:31]\n"
    "v_add_f32 v28, v28, v29\n"
    "v_cvt_pk_bf16_f32 v58, v58, v58\n"
    "v_lshlrev_b32 v58, 16, v58\n"
    "v_add_f32_dpp v28, v28, v28 quad_perm:[1,0,3,2] row_mask:0xf bank_mask:0xf bound_ctrl:1\n"
    "s_nop 1\n"
    "v_add_f32_dpp v28, v28, v28 quad_perm:[2,3,0,1] row_mask:0xf bank_mask:0xf bound_ctrl:1\n"
    "s_nop 1\n"
    "v_add_f32_dpp v28, v28, v28 row_half_mirror row_mask:0xf bank_mask:0xf bound_ctrl:1\n"
    "s_nop 1\n"
    "v_add_f32_dpp v28, v28, v28 row_ror:8 row_mask:0xf bank_mask:0xf bound_ctrl:1\n"
    "v_mov_b32 v29, v28\n"
    "s_nop 1\n"
    "v_permlane16_swap_b32 v28, v29\n"
    "v_add_f32 v28, v28, v29\n"
    "v_pk_add_f32 v[12:13], v[12:13], v[58:59] op_sel_hi:[1,0] neg_lo:[0,1] neg_hi:[0,1]\n"
    "v_pk_add_f32 v[14:15], v[14:15], v[58:59] op_sel_hi:[1,0] neg_lo:[0,1] neg_hi:[0,1]\n"
    "v_pk_add_f32 v[16:17], v[16:17], v[58:59] op_sel_hi:[1,0] neg_lo:[0,1] neg_hi:[0,1]\n"
    "v_pk_add_f32 v[18:19], v[18:19], v[58:59] op_sel_hi:[1,0] neg_lo:[0,1] neg_hi:[0,1]\n"
    "v_pk_add_f32 v[20:21], v[20:21], v[58:59] op_sel_hi:[1,0] neg_lo:[0,1] neg_hi:[0,1]\n"
    "v_pk_add_f32 v[22:23], v[22:23], v[58:59] op_sel_hi:[1,0] neg_lo:[0,1] neg_hi:[0,1]\n"
    "v_pk_add_f32 v[24:25], v[24:25], v[58:59] op_sel_hi:[1,0] neg_lo:[0,1] neg_hi:[0,1]\n"
    "v_pk_add_f32 v[26:27], v[26:27], v[58:59] op_sel_hi:[1,0] neg_lo:[0,1] neg_hi:[0,1]\n"
    "v_mul_f32 v30, 0x3b000000, v28\n"
    "v_cvt_pk_bf16_f32 v30, v30, v30\n"
    "v_lshlrev_b32 v30, 16, v30\n"
    "v_add_f32 v30, 0x3727c5ac, v30\n"
    "v_cvt_pk_bf16_f32 v30, v30, v30\n"
    "v_lshlrev_b32 v30, 16, v30\n"
    "v_sqrt_f32 v30, v30\n"
    "v_cvt_pk_bf16_f32 v4, v12, v13\n"
    "v_cvt_pk_bf16_f32 v5, v14, v15\n"
    "v_cvt_pk_bf16_f32 v6, v16, v17\n"
    "v_cvt_pk_bf16_f32 v7, v18, v19\n"
    "v_cvt_pk_bf16_f32 v8, v20, v21\n"
    "v_cvt_pk_bf16_f32 v9, v22, v23\n"
    "v_cvt_pk_bf16_f32 v10, v24, v25\n"
    "v_cvt_pk_bf16_f32 v11, v26, v27\n"
    "v_cvt_pk_bf16_f32 v30, v30, v30\n"
    "v_lshlrev_b32 v30, 16, v30\n"
    "v_rcp_f32 v56, v30\n"
    "v_lshlrev_b32 v12, 16, v4\n"
    "v_and_b32 v13, 0xffff0000, v4\n"
    "v_lshlrev_b32 v14, 16, v5\n"
    "v_and_b32 v15, 0xffff0000, v5\n"
    "v_lshlrev_b32 v16, 16, v6\n"
    "v_and_b32 v17, 0xffff0000, v6\n"
    "v_lshlrev_b32 v18, 16, v7\n"
    "v_and_b32 v19, 0xffff0000, v7\n"
    "v_lshlrev_b32 v20, 16, v8\n"
    "v_and_b32 v21, 0xffff0000, v8\n"
    "v_lshlrev_b32 v22, 16, v9\n"
    "v_and_b32 v23, 0xffff0000, v9\n"
    "v_lshlrev_b32 v24, 16, v10\n"
    "v_and_b32 v25, 0xffff0000, v10\n"
    "v_lshlrev_b32 v26, 16, v11\n"
    "v_and_b32 v27, 0xffff0000, v11\n"
    "v_pk_mul_f32 v[32:33], v[12:13], v[56:57] op_sel_hi:[1,0]\n"
    "v_pk_mul_f32 v[34:35], v[14:15], v[56:57] op_sel_hi:[1,0]\n"
    "v_pk_mul_f32 v[36:37], v[16:17], v[56:57] op_sel_hi:[1,0]\n"
    "v_pk_mul_f32 v[38:39], v[18:19], v[56:57] op_sel_hi:[1,0]\n"
    "v_pk_mul_f32 v[40:41], v[20:21], v[56:57] op_sel_hi:[1,0]\n"
    "v_pk_mul_f32 v[42:43], v[22:23], v[56:57] op_sel_hi:[1,0]\n"
    "v_pk_mul_f32 v[44:45], v[24:25], v[56:57] op_sel_hi:[1,0]\n"
    "v_pk_mul_f32 v[46:47], v[26:27], v[56:57] op_sel_hi:[1,0]\n"
    "v_pk_fma_f32 v[12:13], v[32:33], v[30:31], v[12:13] op_sel_hi:[1,0,1] neg_lo:[1,0,0] neg_hi:[1,0,0]\n"
    "v_pk_fma_f32 v[14:15], v[34:35], v[30:31], v[14:15] op_sel_hi:[1,0,1] neg_lo:[1,0,0] neg_hi:[1,0,0]\n"
    "v_pk_fma_f32 v[16:17], v[36:37], v[30:31], v[16:17] op_sel_hi:[1,0,1] neg_lo:[1,0,0] neg_hi:[1,0,0]\n"
    "v_pk_fma_f32 v[18:19], v[38:39], v[30:31], v[18:19] op_sel_hi:[1,0,1] neg_lo:[1,0,0] neg_hi:[1,0,0]\n"
    "v_pk_fma_f32 v[20:21], v[40:41], v[30:31], v[20:21] op_sel_hi:[1,0,1] neg_lo:[1,0,0] neg_hi:[1,0,0]\n"
    "v_pk_fma_f32 v[22:23], v[42:43], v[30:31], v[22:23] op_sel_hi:[1,0,1] neg_lo:[1,0,0] neg_hi:[1,0,0]\n"
    "v_pk_fma_f32 v[24:25], v[44:45], v[30:31], v[24:25] op_sel_hi:[1,0,1] neg_lo:[1,0,0] neg_hi:[1,0,0]\n"
    "v_pk_fma_f32 v[26:27], v[46:47], v[30:31], v[26:27] op_sel_hi:[1,0,1] neg_lo:[1,0,0] neg_hi:[1,0,0]\n"
    "v_pk_fma_f32 v[32:33], v[12:13], v[56:57], v[32:33] op_sel_hi:[1,0,1]\n"
    "v_pk_fma_f32 v[34:35], v[14:15], v[56:57], v[34:35] op_sel_hi:[1,0,1]\n"
    "v_pk_fma_f32 v[36:37], v[16:17], v[56:57], v[36:37] op_sel_hi:[1,0,1]\n"
    "v_pk_fma_f32 v[38:39], v[18:19], v[56:57], v[38:39] op_sel_hi:[1,0,1]\n"
    "v_pk_fma_f32 v[40:41], v[20:21], v[56:57], v[40:41] op_sel_hi:[1,0,1]\n"
    "v_pk_fma_f32 v[42:43], v[22:23], v[56:57], v[42:43] op_sel_hi:[1,0,1]\n"
    "v_pk_fma_f32 v[44:45], v[24:25], v[56:57], v[44:45] op_sel_hi:[1,0,1]\n"
    "v_pk_fma_f32 v[46:47], v[26:27], v[56:57], v[46:47] op_sel_hi:[1,0,1]\n"
    "v_cvt_pk_bf16_f32 v4, v32, v33\n"
    "v_cvt_pk_bf16_f32 v5, v34, v35\n"
    "v_cvt_pk_bf16_f32 v6, v36, v37\n"
    "v_cvt_pk_bf16_f32 v7, v38, v39\n"
    "v_cvt_pk_bf16_f32 v8, v40, v41\n"
    "v_cvt_pk_bf16_f32 v9, v42, v43\n"
    "v_cvt_pk_bf16_f32 v10, v44, v45\n"
    "v_cvt_pk_bf16_f32 v11, v46, v47\n"
    "ds_write_b128 v60, v[4:7] offset:1280\n"
    "ds_write_b128 v60, v[8:11] offset:1296\n"
    "v_add_u32 v3, 1, v3\n"
    "v_and_b32 v3, 0x3ff, v3\n"
    "s_cmp_eq_u32 s24, 8\n"
    "s_cbranch_scc0 .Lffn_fd\n"
    "v_lshrrev_b32 v56, 3, v1\n"
    "v_lshlrev_b32 v56, 5, v56\n"
    "s_lshl_b32 s45, s57, 8\n"
    "v_add_u32 v56, s45, v56\n"
    "s_waitcnt lgkmcnt(0)\n"
    "ds_read_b128 v[12:15], v56 offset:1280\n"
    "ds_read_b128 v[20:23], v56 offset:2304\n"
    "ds_read_b128 v[16:19], v56 offset:1296\n"
    "ds_read_b128 v[24:27], v56 offset:2320\n"
    "s_lshl_b32 s44, s57, 10\n"
    "s_lshl_b32 s45, s58, 9\n"
    "s_add_u32 s44, s44, s45\n"
    "s_lshl_b32 s45, s56, 13\n"
    "s_add_u32 s44, s44, s45\n"
    "s_add_u32 s44, s44, 0x1000\n"
    "s_lshl_b32 s46, s25, 13\n"
    "s_add_u32 s46, s46, 0x1000\n"
    "s_lshr_b32 s45, s26, 2\n"
    "s_lshl_b32 s45, s45, 12\n"
    "s_add_u32 s46, s46, s45\n"
    "v_mul_u32_u24 v37, 0x400801, v3\n"
    "v_mov_b32 v39, v37\n"
    "v_and_b32 v57, 7, v1\n"
    "v_lshlrev_b32 v57, 6, v57\n"
    "v_bfe_u32 v58, v1, 4, 1\n"
    "v_lshl_or_b32 v57, v58, 5, v57\n"
    "v_bfe_u32 v58, v1, 3, 1\n"
    "v_lshl_or_b32 v57, v58, 4, v57\n"
    "v_lshrrev_b32 v58, 5, v1\n"
    "v_lshl_or_b32 v57, v58, 12, v57\n"
    "s_waitcnt lgkmcnt(0)\n"
    "v_perm_b32 v28, v12, v12, v48\n"
    "v_perm_b32 v29, v12, v12, v49\n"
    "v_perm_b32 v30, v12, v12, v50\n"
    "v_perm_b32 v31, v12, v12, v51\n"
    "v_perm_b32 v32, v20, v20, v48\n"
    "v_perm_b32 v33, v20, v20, v49\n"
    "v_perm_b32 v34, v20, v20, v50\n"
    "v_perm_b32 v35, v20, v20, v51\n"
    "s_waitcnt vmcnt(15)\n"
    "v_mfma_f32_16x16x32_bf16 v[4:7], v[28:31], v[64:67], 0\n"
    "v_mfma_f32_16x16x32_bf16 v[8:11], v[32:35], v[64:67], 0\n"
    "v_perm_b32 v28, v12, v12, v52\n"
    "v_perm_b32 v29, v12, v12, v53\n"
    "v_perm_b32 v30, v12, v12, v54\n"
    "v_perm_b32 v31, v12, v12, v55\n"
    "v_perm_b32 v32, v20, v20, v52\n"
    "v_perm_b32 v33, v20, v20, v53\n"
    "v_perm_b32 v34, v20, v20, v54\n"
    "v_perm_b32 v35, v20, v20, v55\n"
    "s_waitcnt vmcnt(14)\n"
    "v_mfma_f32_16x16x32_bf16 v[4:7], v[28:31], v[68:71], v[4:7]\n"
    "v_mfma_f32_16x16x32_bf16 v[8:11], v[32:35], v[68:71], v[8:11]\n"
    "v_perm_b32 v28, v13, v13, v48\n"
    "v_perm_b32 v29, v13, v13, v49\n"
    "v_perm_b32 v30, v13, v13, v50\n"
    "v_perm_b32 v31, v13, v13, v51\n"
    "v_perm_b32 v32, v21, v21, v48\n"
    "v_perm_b32 v33, v21, v21, v49\n"
    "v_perm_b32 v34, v21, v21, v50\n"
    "v_perm_b32 v35, v21, v21, v51\n"
    "s_waitcnt vmcnt(13)\n"
    "v_mfma_f32_16x16x32_bf16 v[4:7], v[28:31], v[72:75], v[4:7]\n"
    "v_mfma_f32_16x16x32_bf16 v[8:11], v[32:35], v[72:75], v[8:11]\n"
    "v_perm_b32 v28, v13, v13, v52\n"
    "v_perm_b32 v29, v13, v13, v53\n"
    "v_perm_b32 v30, v13, v13, v54\n"
    "v_perm_b32 v31, v13, v13, v55\n"
    "v_perm_b32 v32, v21, v21, v52\n"
    "v_perm_b32 v33, v21, v21, v53\n"
    "v_perm_b32 v34, v21, v21, v54\n"
    "v_perm_b32 v35, v21, v21, v55\n"
    "s_waitcnt vmcnt(12)\n"
    "v_mfma_f32_16x16x32_bf16 v[4:7], v[28:31], v[76:79], v[4:7]\n"
    "v_mfma_f32_16x16x32_bf16 v[8:11], v[32:35], v[76:79], v[8:11]\n"
    "v_perm_b32 v28, v14, v14, v48\n"
    "v_perm_b32 v29, v14, v14, v49\n"
    "v_perm_b32 v30, v14, v14, v50\n"
    "v_perm_b32 v31, v14, v14, v51\n"
    "v_perm_b32 v32, v22, v22, v48\n"
    "v_perm_b32 v33, v22, v22, v49\n"
    "v_perm_b32 v34, v22, v22, v50\n"
    "v_perm_b32 v35, v22, v22, v51\n"
    "s_waitcnt vmcnt(11)\n"
    "v_mfma_f32_16x16x32_bf16 v[4:7], v[28:31], v[80:83], v[4:7]\n"
    "v_mfma_f32_16x16x32_bf16 v[8:11], v[32:35], v[80:83], v[8:11]\n"
    "v_perm_b32 v28, v14, v14, v52\n"
    "v_perm_b32 v29, v14, v14, v53\n"
    "v_perm_b32 v30, v14, v14, v54\n"
    "v_perm_b32 v31, v14, v14, v55\n"
    "v_perm_b32 v32, v22, v22, v52\n"
    "v_perm_b32 v33, v22, v22, v53\n"
    "v_perm_b32 v34, v22, v22, v54\n"
    "v_perm_b32 v35, v22, v22, v55\n"
    "s_waitcnt vmcnt(10)\n"
    "v_mfma_f32_16x16x32_bf16 v[4:7], v[28:31], v[84:87], v[4:7]\n"
    "v_mfma_f32_16x16x32_bf16 v[8:11], v[32:35], v[84:87], v[8:11]\n"
    "v_perm_b32 v28, v15, v15, v48\n"
    "v_perm_b32 v29, v15, v15, v49\n"
    "v_perm_b32 v30, v15, v15, v50\n"
    "v_perm_b32 v31, v15, v15, v51\n"
    "v_perm_b32 v32, v23, v23, v48\n"
    "v_perm_b32 v33, v23, v23, v49\n"
    "v_perm_b32 v34, v23, v23, v50\n"
    "v_perm_b32 v35, v23, v23, v51\n"
    "s_waitcnt vmcnt(9)\n"
    "v_mfma_f32_16x16x32_bf16 v[4:7], v[28:31], v[88:91], v[4:7]\n"
    "v_mfma_f32_16x16x32_bf16 v[8:11], v[32:35], v[88:91], v[8:11]\n"
    "v_perm_b32 v28, v15, v15, v52\n"
    "v_perm_b32 v29, v15, v15, v53\n"
    "v_perm_b32 v30, v15, v15, v54\n"
    "v_perm_b32 v31, v15, v15, v55\n"
    "v_perm_b32 v32, v23, v23, v52\n"
    "v_perm_b32 v33, v23, v23, v53\n"
    "v_perm_b32 v34, v23, v23, v54\n"
    "v_perm_b32 v35, v23, v23, v55\n"
    "s_waitcnt vmcnt(8)\n"
    "v_mfma_f32_16x16x32_bf16 v[4:7], v[28:31], v[92:95], v[4:7]\n"
    "v_mfma_f32_16x16x32_bf16 v[8:11], v[32:35], v[92:95], v[8:11]\n"
    "v_perm_b32 v28, v16, v16, v48\n"
    "v_perm_b32 v29, v16, v16, v49\n"
    "v_perm_b32 v30, v16, v16, v50\n"
    "v_perm_b32 v31, v16, v16, v51\n"
    "v_perm_b32 v32, v24, v24, v48\n"
    "v_perm_b32 v33, v24, v24, v49\n"
    "v_perm_b32 v34, v24, v24, v50\n"
    "v_perm_b32 v35, v24, v24, v51\n"
    "s_waitcnt vmcnt(7)\n"
    "v_mfma_f32_16x16x32_bf16 v[4:7], v[28:31], v[96:99], v[4:7]\n"
    "v_mfma_f32_16x16x32_bf16 v[8:11], v[32:35], v[96:99], v[8:11]\n"
    "v_perm_b32 v28, v16, v16, v52\n"
    "v_perm_b32 v29, v16, v16, v53\n"
    "v_perm_b32 v30, v16, v16, v54\n"
    "v_perm_b32 v31, v16, v16, v55\n"
    "v_perm_b32 v32, v24, v24, v52\n"
    "v_perm_b32 v33, v24, v24, v53\n"
    "v_perm_b32 v34, v24, v24, v54\n"
    "v_perm_b32 v35, v24, v24, v55\n"
    "s_waitcnt vmcnt(6)\n"
    "v_mfma_f32_16x16x32_bf16 v[4:7], v[28:31], v[100:103], v[4:7]\n"
    "v_mfma_f32_16x16x32_bf16 v[8:11], v[32:35], v[100:103], v[8:11]\n"
    "v_perm_b32 v28, v17, v17, v48\n"
    "v_perm_b32 v29, v17, v17, v49\n"
    "v_perm_b32 v30, v17, v17, v50\n"
    "v_perm_b32 v31, v17, v17, v51\n"
    "v_perm_b32 v32, v25, v25, v48\n"
    "v_perm_b32 v33, v25, v25, v49\n"
    "v_perm_b32 v34, v25, v25, v50\n"
    "v_perm_b32 v35, v25, v25, v51\n"
    "s_waitcnt vmcnt(5)\n"
    "v_mfma_f32_16x16x32_bf16 v[4:7], v[28:31], v[104:107], v[4:7]\n"
    "v_mfma_f32_16x16x32_bf16 v[8:11], v[32:35], v[104:107], v[8:11]\n"
    "v_perm_b32 v28, v17, v17, v52\n"
    "v_perm_b32 v29, v17, v17, v53\n"
    "v_perm_b32 v30, v17, v17, v54\n"
    "v_perm_b32 v31, v17, v17, v55\n"
    "v_perm_b32 v32, v25, v25, v52\n"
    "v_perm_b32 v33, v25, v25, v53\n"
    "v_perm_b32 v34, v25, v25, v54\n"
    "v_perm_b32 v35, v25, v25, v55\n"
    "s_waitcnt vmcnt(4)\n"
    "v_mfma_f32_16x16x32_bf16 v[4:7], v[28:31], v[108:111], v[4:7]\n"
    "v_mfma_f32_16x16x32_bf16 v[8:11], v[32:35], v[108:111], v[8:11]\n"
    "v_perm_b32 v28, v18, v18, v48\n"
    "v_perm_b32 v29, v18, v18, v49\n"
    "v_perm_b32 v30, v18, v18, v50\n"
    "v_perm_b32 v31, v18, v18, v51\n"
    "v_perm_b32 v32, v26, v26, v48\n"
    "v_perm_b32 v33, v26, v26, v49\n"
    "v_perm_b32 v34, v26, v26, v50\n"
    "v_perm_b32 v35, v26, v26, v51\n"
    "s_waitcnt vmcnt(3)\n"
    "v_mfma_f32_16x16x32_bf16 v[4:7], v[28:31], v[112:115], v[4:7]\n"
    "v_mfma_f32_16x16x32_bf16 v[8:11], v[32:35], v[112:115], v[8:11]\n"
    "v_perm_b32 v28, v18, v18, v52\n"
    "v_perm_b32 v29, v18, v18, v53\n"
    "v_perm_b32 v30, v18, v18, v54\n"
    "v_perm_b32 v31, v18, v18, v55\n"
    "v_perm_b32 v32, v26, v26, v52\n"
    "v_perm_b32 v33, v26, v26, v53\n"
    "v_perm_b32 v34, v26, v26, v54\n"
    "v_perm_b32 v35, v26, v26, v55\n"
    "s_waitcnt vmcnt(2)\n"
    "v_mfma_f32_16x16x32_bf16 v[4:7], v[28:31], v[116:119], v[4:7]\n"
    "v_mfma_f32_16x16x32_bf16 v[8:11], v[32:35], v[116:119], v[8:11]\n"
    "v_perm_b32 v28, v19, v19, v48\n"
    "v_perm_b32 v29, v19, v19, v49\n"
    "v_perm_b32 v30, v19, v19, v50\n"
    "v_perm_b32 v31, v19, v19, v51\n"
    "v_perm_b32 v32, v27, v27, v48\n"
    "v_perm_b32 v33, v27, v27, v49\n"
    "v_perm_b32 v34, v27, v27, v50\n"
    "v_perm_b32 v35, v27, v27, v51\n"
    "s_waitcnt vmcnt(1)\n"
    "v_mfma_f32_16x16x32_bf16 v[4:7], v[28:31], v[120:123], v[4:7]\n"
    "v_mfma_f32_16x16x32_bf16 v[8:11], v[32:35], v[120:123], v[8:11]\n"
    "v_perm_b32 v28, v19, v19, v52\n"
    "v_perm_b32 v29, v19, v19, v53\n"
    "v_perm_b32 v30, v19, v19, v54\n"
    "v_perm_b32 v31, v19, v19, v55\n"
    "v_perm_b32 v32, v27, v27, v52\n"
    "v_perm_b32 v33, v27, v27, v53\n"
    "v_perm_b32 v34, v27, v27, v54\n"
    "v_perm_b32 v35, v27, v27, v55\n"
    "s_waitcnt vmcnt(0)\n"
    "v_mfma_f32_16x16x32_bf16 v[4:7], v[28:31], v[124:127], v[4:7]\n"
    "v_mfma_f32_16x16x32_bf16 v[8:11], v[32:35], v[124:127], v[8:11]\n"
    "s_nop 7\n"
    "v_permlane32_swap_b32 v4, v8\n"
    "v_permlane32_swap_b32 v5, v9\n"
    "v_permlane32_swap_b32 v6, v10\n"
    "v_permlane32_swap_b32 v7, v11\n"
    "v_add_f32_dpp v36, v8, v4 row_ror:8 row_mask:0xf bank_mask:0xf bound_ctrl:1\n"
    "v_add_f32_dpp v38, v9, v5 row_ror:8 row_mask:0xf bank_mask:0xf bound_ctrl:1\n"
    "v_add_f32_dpp v40, v10, v6 row_ror:8 row_mask:0xf bank_mask:0xf bound_ctrl:1\n"
    "v_add_f32_dpp v41, v11, v7 row_ror:8 row_mask:0xf bank_mask:0xf bound_ctrl:1\n"
    "s_nop 0\n"
    "v_mov_b32_dpp v36, v40 row_ror:8 row_mask:0xf bank_mask:0xc\n"
    "v_mov_b32_dpp v38, v41 row_ror:8 row_mask:0xf bank_mask:0xc\n"
    "buffer_store_dwordx4 v[36:39], v57, s[40:43], s44 offen sc1\n"
    "s_mov_b32 s48, 0\n"
    "v_lshlrev_b32 v56, 2, v1\n"
    "v_mov_b32 v57, 0\n"
    "v_or_b32 v3, 0x10000, v3\n"
    "s_waitcnt vmcnt(0)\n"
    "s_sleep 12\n"
    ".Lq_fd:\n"
    "buffer_load_dwordx4 v[12:15], v2, s[40:43], s46 offen sc1\n"
    "buffer_load_dwordx4 v[16:19], v2, s[40:43], s46 offen offset:1024 sc1\n"
    "buffer_load_dwordx4 v[20:23], v2, s[40:43], s46 offen offset:2048 sc1\n"
    "buffer_load_dwordx4 v[24:27], v2, s[40:43], s46 offen offset:3072 sc1\n"
    "s_waitcnt vmcnt(0)\n"
    "v_xor_b32 v4, v13, v37\n"
    "v_bitop3_b32 v4, v4, v15, v37 bitop3:0xf6\n"
    "v_bitop3_b32 v4, v4, v17, v37 bitop3:0xf6\n"
    "v_bitop3_b32 v4, v4, v19, v37 bitop3:0xf6\n"
    "v_bitop3_b32 v4, v4, v21, v37 bitop3:0xf6\n"
    "v_bitop3_b32 v4, v4, v23, v37 bitop3:0xf6\n"
    "v_bitop3_b32 v4, v4, v25, v37 bitop3:0xf6\n"
    "v_bitop3_b32 v4, v4, v27, v37 bitop3:0xf6\n"
    "v_cmp_ne_u32 vcc, 0, v4\n"
    "v_add_f32 v5, v12, v16\n"
    "v_add_f32 v6, v20, v24\n"
    "v_add_f32 v7, v14, v18\n"
    "v_add_f32 v8, v22, v26\n"
    "s_cbranch_vccz .Lq_ok_fd\n"
    "s_sleep 16\n"
    "s_add_u32 s48, s48, 1\n"
    "s_cmp_lt_u32 s48, 0x10000\n"
    "s_cbranch_scc1 .Lq_fd\n"
    ".Lq_ok_fd:\n"
    "v_add_f32 v5, v5, v6\n"
    "v_add_f32 v7, v7, v8\n"
    "v_cvt_pk_bf16_f32 v5, v5, v7\n"
    "ds_write_b32 v56, v5 offset:0\n"
    "ds_write_b32 v57, v3 offset:512\n"
    "s_endpgm\n"
    ".Lffn_fd:\n"
    "v_bfe_u32 v58, v1, 3, 1\n"
    "v_lshlrev_b32 v56, 10, v58\n"
    "v_lshl_or_b32 v56, v63, 5, v56\n"
    "s_lshl_b32 s44, s26, 7\n"
    "v_add_u32 v56, s44, v56\n"
    "s_waitcnt lgkmcnt(0)\n"
    "ds_read_b128 v[12:15], v56 offset:1280\n"
    "ds_read_b128 v[16:19], v56 offset:1296\n"
    "v_and_b32 v57, 7, v1\n"
    "v_min_u32 v57, 5, v57\n"
    "v_mad_u32_u24 v57, v58, 6, v57\n"
    "v_mad_u32_u24 v57, v63, 12, v57\n"
    "v_lshlrev_b32 v57, 4, v57\n"
    "s_mul_i32 s46, s25, 6144\n"
    "s_add_u32 s46, s46, 0x41000\n"
    "s_mul_i32 s44, s26, 768\n"
    "s_add_u32 s44, s44, s46\n"
    "s_add_u32 s47, s46, 3072\n"
    "s_movk_i32 s45, 0x3ff\n"
    "v_mul_u32_u24 v20, 0x400801, v3\n"
    "s_lshl_b32 s49, s26, 7\n"
    "s_add_u32 s49, s49, 0xd3000\n"
    "s_cmp_eq_u32 s25, 0\n"
    "s_cbranch_scc0 .Lninit_fd\n"
    "s_lshl_b32 s50, s26, 10\n"
    "s_add_u32 s50, s50, 0xd1000\n"
    "v_mov_b32 v24, 0\n"
    "v_mov_b32 v25, 0\n"
    "v_mov_b32 v26, 0\n"
    "v_mov_b32 v27, 0\n"
    "buffer_store_dwordx4 v[24:27], v2, s[40:43], s50 offen sc1\n"
    "v_mov_b32 v21, v20\n"
    "v_mov_b32 v22, v20\n"
    "v_mov_b32 v23, v20\n"
    ".Lninit_fd:\n"
    "s_waitcnt lgkmcnt(0)\n"
    "v_perm_b32 v28, v12, v12, v48\n"
    "v_perm_b32 v29, v12, v12, v49\n"
    "v_perm_b32 v30, v12, v12, v50\n"
    "v_perm_b32 v31, v12, v12, v51\n"
    "s_waitcnt vmcnt(15)\n"
    "s_nop 0\n"
    "v_mfma_f32_16x16x32_bf16 v[4:7], v[28:31], v[64:67], 0\n"
    "v_perm_b32 v32, v12, v12, v52\n"
    "v_perm_b32 v33, v12, v12, v53\n"
    "v_perm_b32 v34, v12, v12, v54\n"
    "v_perm_b32 v35, v12, v12, v55\n"
    "s_waitcnt vmcnt(14)\n"
    "s_nop 0\n"
    "v_mfma_f32_16x16x32_bf16 v[4:7], v[32:35], v[68:71], v[4:7]\n"
    "v_perm_b32 v28, v13, v13, v48\n"
    "v_perm_b32 v29, v13, v13, v49\n"
    "v_perm_b32 v30, v13, v13, v50\n"
    "v_perm_b32 v31, v13, v13, v51\n"
    "s_waitcnt vmcnt(13)\n"
    "s_nop 0\n"
    "v_mfma_f32_16x16x32_bf16 v[4:7], v[28:31], v[72:75], v[4:7]\n"
    "v_perm_b32 v32, v13, v13, v52\n"
    "v_perm_b32 v33, v13, v13, v53\n"
    "v_perm_b32 v34, v13, v13, v54\n"
    "v_perm_b32 v35, v13, v13, v55\n"
    "s_waitcnt vmcnt(12)\n"
    "s_nop 0\n"
    "v_mfma_f32_16x16x32_bf16 v[4:7], v[32:35], v[76:79], v[4:7]\n"
    "v_perm_b32 v28, v14, v14, v48\n"
    "v_perm_b32 v29, v14, v14, v49\n"
    "v_perm_b32 v30, v14, v14, v50\n"
    "v_perm_b32 v31, v14, v14, v51\n"
    "s_waitcnt vmcnt(11)\n"
    "s_nop 0\n"
    "v_mfma_f32_16x16x32_bf16 v[4:7], v[28:31], v[80:83], v[4:7]\n"
    "v_perm_b32 v32, v14, v14, v52\n"
    "v_perm_b32 v33, v14, v14, v53\n"
    "v_perm_b32 v34, v14, v14, v54\n"
    "v_perm_b32 v35, v14, v14, v55\n"
    "s_waitcnt vmcnt(10)\n"
    "s_nop 0\n"
    "v_mfma_f32_16x16x32_bf16 v[4:7], v[32:35], v[84:87], v[4:7]\n"
    "v_perm_b32 v28, v15, v15, v48\n"
    "v_perm_b32 v29, v15, v15, v49\n"
    "v_perm_b32 v30, v15, v15, v50\n"
    "v_perm_b32 v31, v15, v15, v51\n"
    "s_waitcnt vmcnt(9)\n"
    "s_nop 0\n"
    "v_mfma_f32_16x16x32_bf16 v[4:7], v[28:31], v[88:91], v[4:7]\n"
    "v_perm_b32 v32, v15, v15, v52\n"
    "v_perm_b32 v33, v15, v15, v53\n"
    "v_perm_b32 v34, v15, v15, v54\n"
    "v_perm_b32 v35, v15, v15, v55\n"
    "s_waitcnt vmcnt(8)\n"
    "s_nop 0\n"
    "v_mfma_f32_16x16x32_bf16 v[4:7], v[32:35], v[92:95], v[4:7]\n"
    "v_perm_b32 v28, v16, v16, v48\n"
    "v_perm_b32 v29, v16, v16, v49\n"
    "v_perm_b32 v30, v16, v16, v50\n"
    "v_perm_b32 v31, v16, v16, v51\n"
    "s_waitcnt vmcnt(7)\n"
    "s_nop 0\n"
    "v_mfma_f32_16x16x32_bf16 v[4:7], v[28:31], v[96:99], v[4:7]\n"
    "v_perm_b32 v32, v16, v16, v52\n"
    "v_perm_b32 v33, v16, v16, v53\n"
    "v_perm_b32 v34, v16, v16, v54\n"
    "v_perm_b32 v35, v16, v16, v55\n"
    "s_waitcnt vmcnt(6)\n"
    "s_nop 0\n"
    "v_mfma_f32_16x16x32_bf16 v[4:7], v[32:35], v[100:103], v[4:7]\n"
    "v_perm_b32 v28, v17, v17, v48\n"
    "v_perm_b32 v29, v17, v17, v49\n"
    "v_perm_b32 v30, v17, v17, v50\n"
    "v_perm_b32 v31, v17, v17, v51\n"
    "s_waitcnt vmcnt(5)\n"
    "s_nop 0\n"
    "v_mfma_f32_16x16x32_bf16 v[4:7], v[28:31], v[104:107], v[4:7]\n"
    "v_perm_b32 v32, v17, v17, v52\n"
    "v_perm_b32 v33, v17, v17, v53\n"
    "v_perm_b32 v34, v17, v17, v54\n"
    "v_perm_b32 v35, v17, v17, v55\n"
    "s_waitcnt vmcnt(4)\n"
    "s_nop 0\n"
    "v_mfma_f32_16x16x32_bf16 v[4:7], v[32:35], v[108:111], v[4:7]\n"
    "v_perm_b32 v28, v18, v18, v48\n"
    "v_perm_b32 v29, v18, v18, v49\n"
    "v_perm_b32 v30, v18, v18, v50\n"
    "v_perm_b32 v31, v18, v18, v51\n"
    "s_waitcnt vmcnt(3)\n"
    "s_nop 0\n"
    "v_mfma_f32_16x16x32_bf16 v[4:7], v[28:31], v[112:115], v[4:7]\n"
    "v_perm_b32 v32, v18, v18, v52\n"
    "v_perm_b32 v33, v18, v18, v53\n"
    "v_perm_b32 v34, v18, v18, v54\n"
    "v_perm_b32 v35, v18, v18, v55\n"
    "s_waitcnt vmcnt(2)\n"
    "s_nop 0\n"
    "v_mfma_f32_16x16x32_bf16 v[4:7], v[32:35], v[116:119], v[4:7]\n"
    "v_perm_b32 v28, v19, v19, v48\n"
    "v_perm_b32 v29, v19, v19, v49\n"
    "v_perm_b32 v30, v19, v19, v50\n"
    "v_perm_b32 v31, v19, v19, v51\n"
    "s_waitcnt vmcnt(1)\n"
    "s_nop 0\n"
    "v_mfma_f32_16x16x32_bf16 v[4:7], v[28:31], v[120:123], v[4:7]\n"
    "v_perm_b32 v32, v19, v19, v52\n"
    "v_perm_b32 v33, v19, v19, v53\n"
    "v_perm_b32 v34, v19, v19, v54\n"
    "v_perm_b32 v35, v19, v19, v55\n"
    "s_waitcnt vmcnt(0)\n"
    "s_nop 0\n"
    "v_mfma_f32_16x16x32_bf16 v[4:7], v[32:35], v[124:127], v[4:7]\n"
    "s_nop 7\n"
    "v_bfi_b32 v8, s45, v3, v4\n"
    "v_bfi_b32 v9, s45, v3, v5\n"
    "v_bfi_b32 v10, s45, v3, v6\n"
    "v_bfi_b32 v11, s45, v3, v7\n"
    "s_mov_b32 exec_lo, 0x3f3f3f3f\n"
    "s_mov_b32 exec_hi, 0x3f3f3f3f\n"
    "buffer_store_dwordx4 v[8:11], v57, s[40:43], s44 offen sc1\n"
    "s_mov_b64 exec, -1\n"
    "s_mov_b32 s48, 0\n"
    "v_lshrrev_b32 v56, 5, v1\n"
    "v_lshlrev_b32 v56, 7, v56\n"
    "v_lshl_or_b32 v56, v62, 4, v56\n"
    "v_bfe_u32 v58, v1, 4, 1\n"
    "v_lshl_or_b32 v56, v58, 3, v56\n"
    "v_mov_b32 v12, 0\n"
    "v_mov_b32 v13, 1\n"
    "v_mov_b32 v14, 0\n"
    "v_mov_b32 v15, 2\n"
    "s_lshl_b32 s50, s25, 3\n"
    "s_add_u32 s50, s50, s26\n"
    "s_lshl_b32 s50, s50, 9\n"
    "s_add_u32 s50, s50, 0x71000\n"
    "s_mov_b32 exec_hi, 0\n"
    "buffer_store_dwordx4 v[12:15], v2, s[40:43], s50 offen sc1\n"
    "s_mov_b32 exec_hi, -1\n"
    "s_cmp_eq_u32 s25, 0\n"
    "s_cbranch_scc0 .Lnflag_fd\n"
    "s_mov_b64 exec, 0xff\n"
    "buffer_store_dwordx4 v[20:23], v2, s[40:43], s49 offen sc1\n"
    "s_mov_b64 exec, -1\n"
    ".Lnflag_fd:\n"
    "s_waitcnt vmcnt(0)\n"
    "v_mov_b32 v24, 0\n"
    ".Lfw_fd:\n"
    "s_sleep 2\n"
    "ds_read_b32 v25, v24 offset:520\n"
    "s_waitcnt lgkmcnt(0)\n"
    "v_readfirstlane_b32 s50, v25\n"
    "s_nop 0\n"
    "s_cmp_eq_u32 s50, 8\n"
    "s_cbranch_scc0 .Lfw_fd\n"
    "s_mov_b32 s51, 1\n"
    ".Lf_fd:\n"
    "buffer_load_dword v96, off, s[40:43], s49 sc1\n"
    "buffer_load_dwordx4 v[64:67], v57, s[40:43], s46 offen sc1\n"
    "buffer_load_dwordx4 v[68:71], v57, s[40:43], s46 offen offset:768 sc1\n"
    "buffer_load_dwordx4 v[72:75], v57, s[40:43], s46 offen offset:1536 sc1\n"
    "buffer_load_dwordx4 v[76:79], v57, s[40:43], s46 offen offset:2304 sc1\n"
    "buffer_load_dwordx4 v[80:83], v57, s[40:43], s47 offen sc1\n"
    "buffer_load_dwordx4 v[84:87], v57, s[40:43], s47 offen offset:768 sc1\n"
    "buffer_load_dwordx4 v[88:91], v57, s[40:43], s47 offen offset:1536 sc1\n"
    "buffer_load_dwordx4 v[92:95], v57, s[40:43], s47 offen offset:2304 sc1\n"
    "s_cmp_eq_u32 s51, 0\n"
    "s_cbranch_scc1 .Lfb_fd\n"
    "s_mov_b32 s51, 0\n"
    "s_barrier\n"
    ".Lfb_fd:\n"
    "s_waitcnt vmcnt(7)\n"
    "v_xor_b32 v64, v64, v3\n"
    "v_xor_b32 v65, v65, v3\n"
    "v_xor_b32 v66, v66, v3\n"
    "v_xor_b32 v67, v67, v3\n"
    "v_or3_b32 v4, v64, v65, v66\n"
    "v_or_b32 v4, v4, v67\n"
    "s_waitcnt vmcnt(6)\n"
    "v_xor_b32 v68, v68, v3\n"
    "v_xor_b32 v69, v69, v3\n"
    "v_xor_b32 v70, v70, v3\n"
    "v_xor_b32 v71, v71, v3\n"
    "v_or3_b32 v4, v4, v68, v69\n"
    "v_or3_b32 v4, v4, v70, v71\n"
    "s_waitcnt vmcnt(5)\n"
    "v_xor_b32 v72, v72, v3\n"
    "v_xor_b32 v73, v73, v3\n"
    "v_xor_b32 v74, v74, v3\n"
    "v_xor_b32 v75, v75, v3\n"
    "v_or3_b32 v4, v4, v72, v73\n"
    "v_or3_b32 v4, v4, v74, v75\n"
    "s_waitcnt vmcnt(4)\n"
    "v_xor_b32 v76, v76, v3\n"
    "v_xor_b32 v77, v77, v3\n"
    "v_xor_b32 v78, v78, v3\n"
    "v_xor_b32 v79, v79, v3\n"
    "v_or3_b32 v4, v4, v76, v77\n"
    "v_or3_b32 v4, v4, v78, v79\n"
    "s_waitcnt vmcnt(3)\n"
    "v_xor_b32 v80, v80, v3\n"
    "v_xor_b32 v81, v81, v3\n"
    "v_xor_b32 v82, v82, v3\n"
    "v_xor_b32 v83, v83, v3\n"
    "v_or3_b32 v4, v4, v80, v81\n"
    "v_or3_b32 v4, v4, v82, v83\n"
    "s_waitcnt vmcnt(2)\n"
    "v_xor_b32 v84, v84, v3\n"
    "v_xor_b32 v85, v85, v3\n"
    "v_xor_b32 v86, v86, v3\n"
    "v_xor_b32 v87, v87, v3\n"
    "v_or3_b32 v4, v4, v84, v85\n"
    "v_or3_b32 v4, v4, v86, v87\n"
    "s_waitcnt vmcnt(1)\n"
    "v_xor_b32 v88, v88, v3\n"
    "v_xor_b32 v89, v89, v3\n"
    "v_xor_b32 v90, v90, v3\n"
    "v_xor_b32 v91, v91, v3\n"
    "v_or3_b32 v4, v4, v88, v89\n"
    "v_or3_b32 v4, v4, v90, v91\n"
    "s_waitcnt vmcnt(0)\n"
    "v_xor_b32 v92, v92, v3\n"
    "v_xor_b32 v93, v93, v3\n"
    "v_xor_b32 v94, v94, v3\n"
    "v_xor_b32 v95, v95, v3\n"
    "v_or3_b32 v4, v4, v92, v93\n"
    "v_or3_b32 v4, v4, v94, v95\n"
    "v_and_b32 v4, s45, v4\n"
    "v_bitop3_b32 v4, v4, v96, v20 bitop3:0xf6\n"
    "v_cmp_ne_u32 vcc, 0, v4\n"
    "s_cbranch_vccz .Lf_ok_fd\n"
    "s_add_u32 s48, s48, 1\n"
    "s_cmp_lt_u32 s48, 0x10000\n"
    "s_cbranch_scc1 .Lf_fd\n"
    ".Lf_ok_fd:\n"
    "v_pk_add_f32 v[64:65], v[64:65], v[68:69]\n"
    "v_pk_add_f32 v[66:67], v[66:67], v[70:71]\n"
    "v_pk_add_f32 v[64:65], v[64:65], v[72:73]\n"
    "v_pk_add_f32 v[66:67], v[66:67], v[74:75]\n"
    "v_pk_add_f32 v[64:65], v[64:65], v[76:77]\n"
    "v_pk_add_f32 v[66:67], v[66:67], v[78:79]\n"
    "v_pk_add_f32 v[64:65], v[64:65], v[80:81]\n"
    "v_pk_add_f32 v[66:67], v[66:67], v[82:83]\n"
    "v_pk_add_f32 v[64:65], v[64:65], v[84:85]\n"
    "v_pk_add_f32 v[66:67], v[66:67], v[86:87]\n"
    "v_pk_add_f32 v[64:65], v[64:65], v[88:89]\n"
    "v_pk_add_f32 v[66:67], v[66:67], v[90:91]\n"
    "v_pk_add_f32 v[64:65], v[64:65], v[92:93]\n"
    "v_pk_add_f32 v[66:67], v[66:67], v[94:95]\n"
    "v_cvt_pk_bf16_f32 v64, v64, v64\n"
    "v_lshlrev_b32 v64, 16, v64\n"
    "v_cvt_pk_bf16_f32 v65, v65, v65\n"
    "v_lshlrev_b32 v65, 16, v65\n"
    "v_cvt_pk_bf16_f32 v66, v66, v66\n"
    "v_lshlrev_b32 v66, 16, v66\n"
    "v_cvt_pk_bf16_f32 v67, v67, v67\n"
    "v_lshlrev_b32 v67, 16, v67\n"
    "v_mul_f32 v4, v64, v64\n"
    "v_mul_f32 v5, v65, v65\n"
    "v_mul_f32 v6, v66, v66\n"
    "v_mul_f32 v7, v67, v67\n"
    "v_mul_f32 v4, v4, v64\n"
    "v_mul_f32 v5, v5, v65\n"
    "v_mul_f32 v6, v6, v66\n"
    "v_mul_f32 v7, v7, v67\n"
    "v_mul_f32 v4, 0x3d372713, v4\n"
    "v_mul_f32 v5, 0x3d372713, v5\n"
    "v_mul_f32 v6, 0x3d372713, v6\n"
    "v_mul_f32 v7, 0x3d372713, v7\n"
    "v_add_f32 v4, v4, v64\n"
    "v_add_f32 v5, v5, v65\n"
    "v_add_f32 v6, v6, v66\n"
    "v_add_f32 v7, v7, v67\n"
    "v_mul_f32 v4, 0xc0135761, v4\n"
    "v_mul_f32 v5, 0xc0135761, v5\n"
    "v_mul_f32 v6, 0xc0135761, v6\n"
    "v_mul_f32 v7, 0xc0135761, v7\n"
    "v_exp_f32 v4, v4\n"
    "v_exp_f32 v5, v5\n"
    "v_exp_f32 v6, v6\n"
    "v_exp_f32 v7, v7\n"
    "v_add_f32 v4, 1.0, v4\n"
    "v_add_f32 v5, 1.0, v5\n"
    "v_add_f32 v6, 1.0, v6\n"
    "v_add_f32 v7, 1.0, v7\n"
    "v_rcp_f32 v4, v4\n"
    "v_rcp_f32 v5, v5\n"
    "v_rcp_f32 v6, v6\n"
    "v_rcp_f32 v7, v7\n"
    "v_mul_f32 v4, v4, v64\n"
    "v_mul_f32 v5, v5, v65\n"
    "v_mul_f32 v6, v6, v66\n"
    "v_mul_f32 v7, v7, v67\n"
    "v_cvt_pk_bf16_f32 v4, v4, v4\n"
    "v_lshlrev_b32 v4, 16, v4\n"
    "v_cvt_pk_bf16_f32 v5, v5, v5\n"
    "v_lshlrev_b32 v5, 16, v5\n"
    "v_cvt_pk_bf16_f32 v6, v6, v6\n"
    "v_lshlrev_b32 v6, 16, v6\n"
    "v_cvt_pk_bf16_f32 v7, v7, v7\n"
    "v_lshlrev_b32 v7, 16, v7\n"
    "v_mul_f32_dpp v4, v64, v4 row_ror:8 row_mask:0xf bank_mask:0xf bound_ctrl:1\n"
    "v_mul_f32_dpp v5, v65, v5 row_ror:8 row_mask:0xf bank_mask:0xf bound_ctrl:1\n"
    "v_mul_f32_dpp v6, v66, v6 row_ror:8 row_mask:0xf bank_mask:0xf bound_ctrl:1\n"
    "v_mul_f32_dpp v7, v67, v7 row_ror:8 row_mask:0xf bank_mask:0xf bound_ctrl:1\n"
    "v_cvt_pk_bf16_f32 v4, v4, v5\n"
    "v_cvt_pk_bf16_f32 v5, v6, v7\n"
    "v_cmp_gt_u32 vcc, s3, v62\n"
    "s_nop 1\n"
    "v_cndmask_b32 v4, 0, v4, vcc\n"
    "v_cndmask_b32 v5, 0, v5, vcc\n"
    "s_mov_b32 exec_lo, 0x00ff00ff\n"
    "s_mov_b32 exec_hi, 0x00ff00ff\n"
    "ds_write_b64 v56, v[4:5] offset:256\n"
    "v_mov_b32 v25, 1\n"
    "s_mov_b64 exec, 1\n"
    "ds_add_u32 v24, v25 offset:516\n"
    "s_endpgm\n"
    ".Lend_fd:\n"
    :: "{s[0:1]}"(__builtin_amdgcn_kernarg_segment_ptr()), "{s2}"(__builtin_amdgcn_workgroup_id_x()), "{v0}"(__builtin_amdgcn_workitem_id_x()),
       "s"((__attribute__((address_space(3))) unsigned char*)lds)
    : "v1", "v2", "v3", "v4", "v5", "v6", "v7", "v8", "v9", "v10", "v11", "v12", "v13", "v14", "v15", "v16", "v17", "v18", "v19", "v20", "v21", "v22", "v23", "v24", "v25", "v26", "v27", "v28", "v29", "v30", "v31", "v32", "v33", "v34", "v35", "v36", "v37", "v38", "v39", "v40", "v41", "v42", "v43", "v44", "v45", "v46", "v47", "v48", "v49", "v50", "v51", "v52", "v53", "v54", "v55", "v56", "v57", "v58", "v59", "v60", "v61", "v62", "v63", "v64", "v65", "v66", "v67", "v68", "v69", "v70", "v71", "v72", "v73", "v74", "v75", "v76", "v77", "v78", "v79", "v80", "v81", "v82", "v83", "v84", "v85", "v86", "v87", "v88", "v89", "v90", "v91", "v92", "v93", "v94", "v95", "v96", "v97", "v98", "v99", "v100", "v101", "v102", "v103", "v104", "v105", "v106", "v107", "v108", "v109", "v110", "v111", "v112", "v113", "v114", "v115", "v116", "v117", "v118", "v119", "v120", "v121", "v122", "v123", "v124", "v125", "v126", "v127", "s3", "s4", "s5", "s6", "s7", "s8", "s9", "s10", "s11", "s12", "s13", "s14", "s15", "s16", "s17", "s18", "s19", "s20", "s21", "s22", "s23", "s24", "s25", "s26", "s27", "s28", "s29", "s30", "s31", "s33", "s34", "s35", "s36", "s37", "s38", "s39", "s40", "s41", "s42", "s43", "s44", "s45", "s46", "s47", "s48", "s49", "s50", "s51", "s52", "s53", "s54", "s55", "s56", "s57", "s58", "s59", "s60", "s61", "s62", "s63", "s64", "vcc", "scc", "memory");
}

extern "C" void kernel_launch(void* const* d_in, const int* in_sizes, int n_in, void* d_out, int out_size, void* d_ws, size_t ws_size,
                              hipStream_t stream) {
  fused_decode<<<256, 640, 0, stream>>>(d_in[0], d_in[1], d_in[2], d_in[3], d_in[4], d_in[6], d_in[7], d_in[8], d_out, d_ws);
}
```
